```python
import torch

def _softmax(x, axis=-1):
    return torch.softmax(x, dim=axis)
def _gelu(x):
    return torch.nn.functional.gelu(x, approximate="tanh")
def _silu(x):
    return torch.nn.functional.silu(x)
def _sigmoid(x):
    return torch.sigmoid(x)
def _layernorm(x, axis=-1, eps=1e-5):
    m = x.mean(dim=axis, keepdim=True)
    v = x.var(dim=axis, keepdim=True, unbiased=False)
    return (x - m) / torch.sqrt(v + eps)
def _scatter_add(tgt, idx, src, axis=0):
    return tgt.clone().index_add_(axis, idx, src)
def _segment_sum(src, seg, n):
    out = torch.zeros((n,) + src.shape[1:], dtype=src.dtype)
    return out.index_add_(0, seg, src)
def _pad(x, pw):
    flat = tuple(v for pair in reversed(pw) for v in pair)
    return torch.nn.functional.pad(x, flat)

kernel_name = 'gated_mix__mha_block__dense_activation_squared_relu__prompt'

def setup_inputs():

    _w = (lambda a: (lambda t: t.to(torch.bfloat16) if t.is_floating_point() else t)(torch.as_tensor(a)))
    def _A_setup():
        B, S, D, H = 1, 2048, 512, 32
        return {
            "x": _w((lambda *s: torch.randn(*s, dtype=torch.bfloat16) * 0.05)(B, S, D)),
            "wqkv": _w((lambda *s: torch.randn(*s, dtype=torch.bfloat16) * 0.8)(D, 3 * D)),
            "wo": _w((lambda *s: torch.randn(*s, dtype=torch.bfloat16) * 0.1)(D, D)),
        }
    _A_in = _A_setup()

    def _B_setup():
        B, S, D, F = 1, 2048, 512, 1792
        return {
            "x": _w((lambda *s: torch.randn(*s, dtype=torch.bfloat16) * 0.05)(B, S, D)),
            "w1": _w((lambda *s: torch.randn(*s, dtype=torch.bfloat16) * 0.1)(D, F)),
            "b1": _w((lambda *s: torch.randn(*s, dtype=torch.bfloat16) * 0.05)(F)),
            "w2": _w((lambda *s: torch.randn(*s, dtype=torch.bfloat16) * 0.05)(F, D)),
            "b2": _w((lambda *s: torch.randn(*s, dtype=torch.bfloat16) * 0.05)(D)),
        }
    _B_in = _B_setup()

    return {
        "A_x": _A_in["x"],
        "A_wqkv": _A_in["wqkv"],
        "A_wo": _A_in["wo"],
        "B_x": _B_in["x"],
        "B_w1": _B_in["w1"],
        "B_b1": _B_in["b1"],
        "B_w2": _B_in["w2"],
        "B_b2": _B_in["b2"],
        "gate_w": _w((lambda *s: torch.randn(*s, dtype=torch.bfloat16) * 0.05)(512) * 16.0),
    }

def reference(A_x, A_wqkv, A_wo, B_x, B_w1, B_b1, B_w2, B_b2, gate_w):
    def _A_ref(x, wqkv, wo):
        B, S, D = x.shape
        H = 32
        Dh = D // H
        qkv = (lambda x, *d: x.permute(*d))((x @ wqkv).reshape(B, S, 3, H, Dh), 2, 0, 3, 1, 4)
        q, k, v = qkv[0], qkv[1], qkv[2]
        scores = torch.einsum("bhqd,bhkd->bhqk", q, k) * (Dh ** -0.5)
        p = _softmax(scores, axis=-1)
        o = torch.einsum("bhqk,bhkd->bhqd", p, v)
        return (lambda x, *d: x.permute(*d))(o, 0, 2, 1, 3).reshape(B, S, D) @ wo

    def _B_ref(x, w1, b1, w2, b2):
        h = torch.einsum("bsd,df->bsf", x, w1) + b1
        zero = torch.zeros_like(h)
        r = torch.where(h > zero, h, zero)
        h = r * r
        out = torch.einsum("bsf,fd->bsd", h, w2) + b2
        return out

    x = A_x
    g = _sigmoid((x * gate_w).sum(-1, keepdims=True))
    a = _A_ref(x, A_wqkv, A_wo)
    b = _B_ref(x, B_w1, B_b1, B_w2, B_b2)
    return g * a + (1 - g) * b
```

```cpp
#include <hip/hip_runtime.h>
#include <hip/hip_bf16.h>
typedef unsigned short u16;
__device__ inline float b2f(u16 v) { return __uint_as_float(((unsigned)v) << 16); }
__device__ inline u16 f2b(float f) { __hip_bfloat16 b = __float2bfloat16(f); return *(u16*)&b; }

template<bool AF32>
__global__ void gemm_naive(const void* A, const u16* B, float* C, int M, int N, int K) {
  __shared__ float As[16][17], Bs[16][17];
  int tx = threadIdx.x & 15, ty = threadIdx.x >> 4;
  int r = blockIdx.y * 16 + ty, c = blockIdx.x * 16 + tx;
  float acc = 0;
  for (int k0 = 0; k0 < K; k0 += 16) {
    As[ty][tx] = AF32 ? ((const float*)A)[r * K + k0 + tx] : b2f(((const u16*)A)[r * K + k0 + tx]);
    Bs[ty][tx] = b2f(B[(k0 + ty) * N + c]);
    __syncthreads();
    for (int k = 0; k < 16; ++k) acc += As[ty][k] * Bs[k][tx];
    __syncthreads();
  }
  C[r * N + c] = acc;
}
__global__ void attn_naive(const float* qkv, float* o) {
  int h = blockIdx.y, q = blockIdx.x * 64 + threadIdx.x;
  float qv[16], acc[16];
  for (int d = 0; d < 16; ++d) { qv[d] = qkv[q * 1536 + h * 16 + d] * 0.25f; acc[d] = 0; }
  float m = -1e30f, l = 0;
  for (int k = 0; k < 2048; ++k) {
    float s = 0;
    for (int d = 0; d < 16; ++d) s += qv[d] * qkv[k * 1536 + 512 + h * 16 + d];
    float mn = fmaxf(m, s), a = __expf(m - mn), p = __expf(s - mn);
    l = l * a + p;
    for (int d = 0; d < 16; ++d) acc[d] = acc[d] * a + p * qkv[k * 1536 + 1024 + h * 16 + d];
    m = mn;
  }
  for (int d = 0; d < 16; ++d) o[q * 512 + h * 16 + d] = acc[d] / l;
}
__global__ void act_naive(float* h, const u16* b1) {
  int i = blockIdx.x * 256 + threadIdx.x;
  float v = h[i] + b2f(b1[i % 1792]);
  v = v > 0 ? v : 0;
  h[i] = v * v;
}
__global__ void fin_naive(const u16* x, const u16* gw, const float* a, const float* b, const u16* b2, u16* out) {
  int r = blockIdx.x;
  __shared__ float red[256];
  float s = 0;
  for (int c = threadIdx.x; c < 512; c += 256) s += b2f(x[r * 512 + c]) * b2f(gw[c]);
  red[threadIdx.x] = s; __syncthreads();
  for (int o = 128; o > 0; o >>= 1) { if (threadIdx.x < o) red[threadIdx.x] += red[threadIdx.x + o]; __syncthreads(); }
  float g = 1.f / (1.f + __expf(-red[0]));
  for (int c = threadIdx.x; c < 512; c += 256) out[r * 512 + c] = f2b(g * a[r * 512 + c] + (1.f - g) * (b[r * 512 + c] + b2f(b2[c])));
}
extern "C" void kernel_launch(void* const* d_in, const int* in_sizes, int n_in, void* d_out, int out_size, void* d_ws, size_t ws_size, hipStream_t stream) {
  const u16* x = (const u16*)d_in[0]; const u16* wqkv = (const u16*)d_in[1]; const u16* wo = (const u16*)d_in[2];
  const u16* w1 = (const u16*)d_in[4]; const u16* b1 = (const u16*)d_in[5]; const u16* w2 = (const u16*)d_in[6]; const u16* b2 = (const u16*)d_in[7];
  const u16* gw = (const u16*)d_in[8];
  float* qkv = (float*)d_ws; float* o = qkv + 2048 * 1536; float* h = o + 2048 * 512; float* a = h + 2048 * 1792; float* b = a + 2048 * 512;
  gemm_naive<false><<<dim3(1536 / 16, 128), 256, 0, stream>>>(x, wqkv, qkv, 2048, 1536, 512);
  attn_naive<<<dim3(32, 32), 64, 0, stream>>>(qkv, o);
  gemm_naive<true><<<dim3(512 / 16, 128), 256, 0, stream>>>(o, wo, a, 2048, 512, 512);
  gemm_naive<false><<<dim3(1792 / 16, 128), 256, 0, stream>>>(x, w1, h, 2048, 1792, 512);
  act_naive<<<2048 * 1792 / 256, 256, 0, stream>>>(h, b1);
  gemm_naive<true><<<dim3(512 / 16, 128), 256, 0, stream>>>(h, w2, b, 2048, 512, 1792);
  fin_naive<<<2048, 256, 0, stream>>>(x, gw, a, b, b2, (u16*)d_out);
}
```

```cpp
#include <hip/hip_runtime.h>
#include <stdint.h>

typedef __attribute__((ext_vector_type(8))) __bf16 bf16x8;
typedef __attribute__((ext_vector_type(4))) __bf16 bf16x4;
typedef __attribute__((ext_vector_type(2))) __bf16 bf16x2;
typedef __attribute__((ext_vector_type(2))) float f32x2;
typedef __attribute__((ext_vector_type(4))) float f32x4;
typedef __attribute__((ext_vector_type(16))) float f32x16;
typedef __attribute__((ext_vector_type(4))) unsigned u32x4;
typedef __attribute__((ext_vector_type(2))) unsigned u32x2;
typedef unsigned short u16;
#define LAS __attribute__((address_space(3)))
#define DI __device__ __forceinline__
#define SB __builtin_amdgcn_sched_barrier(0)
#define MFMA(a, b, c) __builtin_amdgcn_mfma_f32_32x32x16_bf16(__builtin_bit_cast(bf16x8, a), __builtin_bit_cast(bf16x8, b), (c), 0, 0, 0)

constexpr unsigned WS_E = 0, WS_ARM = 1024, WS_HF = 4096, WS_KN = 8192, WS_G = 16384, WS_DBG = 65536;
constexpr unsigned WS_KV = 1u << 20, WS_O = 5u << 20, WS_H = 7u << 20, WS_BYTES = 14u << 20;
constexpr unsigned OOB = 0x7f000000u;
constexpr unsigned L_X = 0, L_WR = 131072, L_QX = 131072, L_W1 = 131072, L_MISC = 159744;
constexpr unsigned L_R = 0, L_W2 = 65536;
constexpr unsigned M_TAG = L_MISC, M_MSYNC = L_MISC + 4, M_ADONE = L_MISC + 8, M_RSYNC = L_MISC + 16, M_QMAX = L_MISC + 24,
                   M_KMAX = L_MISC + 32, M_SLOW = L_MISC + 64;
constexpr int SPIN_MAX = 1 << 14;
constexpr float SAFE2 = 40.f * 40.f;

DI u32x4 make_srd(const void* p, unsigned bytes) {
  uint64_t a = (uint64_t)p;
  u32x4 r = {(unsigned)a, (unsigned)(a >> 32) & 0xffffu, bytes, 0x00020000u};
  return r;
}
DI void dma16(u32x4 srd, unsigned voff, unsigned soff, unsigned ldsaddr) {
  asm volatile("s_mov_b32 m0, %3\n\ts_nop 0\n\tbuffer_load_dwordx4 %0, %1, %2 offen lds" ::"v"(voff), "s"(srd), "s"(soff), "s"(ldsaddr) : "memory");
}
DI void ld16(u32x4& d, u32x4 srd, unsigned voff, unsigned soff) {
  asm volatile("buffer_load_dwordx4 %0, %1, %2, %3 offen" : "=v"(d) : "v"(voff), "s"(srd), "s"(soff) : "memory");
}
DI void ld16c(u32x4& d, u32x4 srd, unsigned voff, unsigned soff) {
  asm volatile("buffer_load_dwordx4 %0, %1, %2, %3 offen sc1" : "=v"(d) : "v"(voff), "s"(srd), "s"(soff) : "memory");
}
DI void ld4c(unsigned& d, u32x4 srd, unsigned voff, unsigned soff) {
  asm volatile("buffer_load_dword %0, %1, %2, %3 offen sc1" : "=v"(d) : "v"(voff), "s"(srd), "s"(soff) : "memory");
}
DI unsigned ld4c_wait(u32x4 srd, unsigned voff, unsigned soff) {
  unsigned d;
  asm volatile("buffer_load_dword %0, %1, %2, %3 offen sc1\n\ts_waitcnt vmcnt(0)" : "=&v"(d) : "v"(voff), "s"(srd), "s"(soff) : "memory");
  return d;
}
DI void st16c(u32x4 v, u32x4 srd, unsigned voff, unsigned soff) {
  asm volatile("buffer_store_dwordx4 %0, %1, %2, %3 offen sc1\n\ts_nop 1" ::"v"(v), "v"(voff), "s"(srd), "s"(soff) : "memory");
}
DI void st16(u32x4 v, u32x4 srd, unsigned voff, unsigned soff) {
  asm volatile("buffer_store_dwordx4 %0, %1, %2, %3 offen\n\ts_nop 1" ::"v"(v), "v"(voff), "s"(srd), "s"(soff) : "memory");
}
DI void st4c(unsigned v, u32x4 srd, unsigned voff, unsigned soff) {
  asm volatile("buffer_store_dword %0, %1, %2, %3 offen sc1" ::"v"(v), "v"(voff), "s"(srd), "s"(soff) : "memory");
}
#define VMCNT(n) asm volatile("s_waitcnt vmcnt(" #n ")" ::: "memory")
#define LGKM0 asm volatile("s_waitcnt lgkmcnt(0)" ::: "memory")
DI void bar() { asm volatile("s_waitcnt lgkmcnt(0)\n\ts_barrier" ::: "memory"); }

DI unsigned pk2(float a, float b) { f32x2 v = {a, b}; return __builtin_bit_cast(unsigned, __builtin_convertvector(v, bf16x2)); }
DI unsigned nosent(unsigned v) { return v < 0xfffffffeu ? v : 0xfffffffeu; }
template <int O> DI u32x4 pack8(const f32x16& a) {
  u32x4 r = {pk2(a[O], a[O + 1]), pk2(a[O + 2], a[O + 3]), pk2(a[O + 4], a[O + 5]), pk2(a[O + 6], a[O + 7])};
  return r;
}
DI u32x4 nosent4(u32x4 v) { u32x4 r = {nosent(v[0]), nosent(v[1]), nosent(v[2]), nosent(v[3])}; return r; }
DI unsigned umax3(unsigned a, unsigned b, unsigned c) { unsigned m = a > b ? a : b; return m > c ? m : c; }
DI unsigned umax4(u32x4 v, unsigned m) { return umax3(umax3(m, v[0], v[1]), v[2], v[3]); }
DI float bflo(unsigned v) { return __builtin_bit_cast(float, v << 16); }
DI float bfhi(unsigned v) { return __builtin_bit_cast(float, v & 0xffff0000u); }
DI int fresh_lane() { int l; asm volatile("v_mbcnt_lo_u32_b32 %0, -1, 0\n\tv_mbcnt_hi_u32_b32 %0, -1, %0" : "=v"(l)); return l; }
DI float xhalf(float v, int lane) {
  unsigned u = __builtin_bit_cast(unsigned, v);
  auto r = __builtin_amdgcn_permlane32_swap(u, u, false, false);
  return __builtin_bit_cast(float, (lane & 32) ? r[0] : r[1]);
}

__shared__ __attribute__((aligned(16))) char lds[163840];
template <class T> DI T* lp(unsigned a) { return (T*)(lds + a); }
DI u32x4 lrd16(unsigned a) { return *lp<u32x4>(a); }
DI void lwr16(unsigned a, u32x4 v) { *lp<u32x4>(a) = v; }
DI u32x4 ltr(unsigned a, unsigned d) {
  bf16x4 lo = __builtin_amdgcn_ds_read_tr16_b64_v4bf16((LAS bf16x4*)(lds + a));
  bf16x4 hi = __builtin_amdgcn_ds_read_tr16_b64_v4bf16((LAS bf16x4*)(lds + a + d));
  return __builtin_bit_cast(u32x4, __builtin_shufflevector(lo, hi, 0, 1, 2, 3, 4, 5, 6, 7));
}
DI unsigned lword(unsigned a) { return __builtin_amdgcn_readfirstlane(__hip_atomic_load(lp<unsigned>(a), __ATOMIC_RELAXED, __HIP_MEMORY_SCOPE_WORKGROUP)); }
DI void larrive(unsigned a) {
  asm volatile("s_waitcnt lgkmcnt(0)\n\ts_mov_b64 exec, 1\n\tds_add_u32 %0, %1\n\ts_mov_b64 exec, -1" ::"v"(a), "v"(1u) : "memory");
}
template <int SLP> DI void lwait(unsigned a, unsigned target) {
#pragma unroll 1
  for (int i = 0; i < (SPIN_MAX << 4); ++i) {
    if (lword(a) >= target) break;
    __builtin_amdgcn_s_sleep(SLP);
  }
  asm volatile("" ::: "memory");
}


DI void kv_load(u32x4& K, u32x4& V0, u32x4& V1, u32x4 srd, unsigned voff, unsigned soff) {
  asm volatile(
      "buffer_load_dwordx4 %0, %3, %4, %5 offen sc1\n\t"
      "s_mov_b64 exec, %6\n\t"
      "buffer_load_dwordx4 %1, %3, %4, %5 offen offset:1024 sc1\n\t"
      "buffer_load_dwordx4 %2, %3, %4, %5 offen offset:1280 sc1\n\t"
      "s_mov_b64 exec, -1"
      : "+v"(K), "+v"(V0), "+v"(V1)
      : "v"(voff), "s"(srd), "s"(soff), "s"(0x0000ffff0000ffffull)
      : "memory");
}
__global__ void __launch_bounds__(768, 3)
gm_kernel(const u16* x, const u16* wqkv, const u16* wo, const u16* w1, const u16* b1, const u16* w2, const u16* b2,
          const u16* gw, u16* out, void* ws) {
  const int lane = threadIdx.x & 63;
  const int w = __builtin_amdgcn_readfirstlane(threadIdx.x >> 6);
  const int xcd = blockIdx.x & 7, idx = blockIdx.x >> 3;
  const int t = (xcd & 1) * 8 + (idx & 7), p = (xcd >> 1) * 4 + (idx >> 3);
  const int rt = xcd * 4 + (idx >> 3), ct = idx & 7;
  const u32x4 sws = make_srd(ws, WS_BYTES);
  const int r = lane & 31, hh = lane >> 5;
  const unsigned l16 = lane * 16;
  const unsigned xbase = r * 128 + ((hh ^ ((r >> 1) & 7)) << 4);
  const int tq = (lane & 15) >> 2, tp = lane & 3, tblk = (lane >> 4) & 1;
  asm volatile("s_nop 4");

  if (w < 8) {
    const int tb = w & 3, hd = w >> 2, head = 2 * p + hd;
    {
      const u32x4 sx = make_srd(x, 2048 * 512 * 2);
      unsigned v0 = (lane >> 3) * 1024 + (((lane & 7) ^ ((lane >> 4) & 7)) << 4);
      unsigned v1 = (8 + (lane >> 3)) * 1024 + (((lane & 7) ^ ((4 + (lane >> 4)) & 7)) << 4);
      const unsigned so = (t * 128 + w * 16) * 1024;
#pragma unroll
      for (int c = 0; c < 8; ++c) {
        dma16(sx, v0, so + c * 128, L_X + c * 16384 + w * 2048);
        dma16(sx, v1, so + c * 128, L_X + c * 16384 + w * 2048 + 1024);
      }
    }
    f32x16 aqk, av;
#pragma unroll
    for (int i = 0; i < 16; ++i) { aqk[i] = 0.f; av[i] = 0.f; }
    {
      const unsigned wb = (8 * hh + tq) * 64 + (tblk ^ hd) * 32 + tp * 8;
      const unsigned wqk = L_WR + wb + hd * 4096, wv = L_WR + wb + 8192;
      const unsigned xa = L_X + tb * 4096 + xbase;
#pragma unroll
      for (int c = 0; c < 8; ++c) {
        asm volatile("s_waitcnt vmcnt(%0)" ::"i"(14 - 2 * c) : "memory");
        bar();
#pragma unroll
        for (int s = 0; s < 4; ++s) {
          u32x4 xf = lrd16((xa ^ (s << 5)) + c * 16384);
          u32x4 fq = ltr(wqk + (c & 1) * 12288 + s * 1024, 256);
          u32x4 fv = ltr(wv + (c & 1) * 12288 + s * 1024, 256);
          aqk = MFMA(fq, xf, aqk);
          av = MFMA(xf, fv, av);
        }
      }
    }
    u32x4 KA, VA0, VA1, Q0, Q1, Q2, Q3;
    const unsigned kvh = WS_KV + head * 64 * 2048 + tb * 2048;
    const u32x4 ones = {0x3f803f80u, 0x3f803f80u, 0x3f803f80u, 0x3f803f80u};
    {
      KA = nosent4(pack8<8>(aqk));
      st16c(KA, sws, l16, kvh + t * 8192);
      VA0 = nosent4(pack8<0>(av));
      VA1 = nosent4(pack8<8>(av));
      if (r < 16) {
        st16c(VA0, sws, l16 + 1024, kvh + t * 8192);
        st16c(VA1, sws, l16 + 1280, kvh + t * 8192);
      }
      const float qs = 0.25f * 1.4426950408889634f;
      float qn = 0.f, kn = 0.f, vm = 0.f;
#pragma unroll
      for (int i = 0; i < 8; ++i) { aqk[i] *= qs; qn += aqk[i] * aqk[i]; kn += aqk[8 + i] * aqk[8 + i]; }
      Q0 = pack8<0>(aqk);
      lwr16(L_QX + w * 1024 + l16, Q0);
#pragma unroll
      for (int i = 0; i < 16; ++i) vm = fmaxf(vm, fabsf(av[i]));
      qn += xhalf(qn, lane);
      kn += xhalf(kn, lane);
      if (r < 16 && !(vm <= 1e12f)) kn = __builtin_inff();
      asm volatile("ds_max_u32 %0, %1\n\tds_max_u32 %2, %3" ::"v"(M_QMAX + hd * 4), "v"(qn), "v"(M_KMAX + w * 4), "v"(kn) : "memory");
      if (r >= 16) { VA0 = ones; VA1 = ones; }
    }
    bar();
    Q1 = lrd16(L_QX + hd * 4096 + ((tb + 1) & 3) * 1024 + l16);
    Q2 = lrd16(L_QX + hd * 4096 + ((tb + 2) & 3) * 1024 + l16);
    Q3 = lrd16(L_QX + hd * 4096 + ((tb + 3) & 3) * 1024 + l16);
    unsigned km = lword(M_KMAX + w * 4);
    km = km < 0x7f800000u ? km : 0x7f800000u;
    bar();

    f32x16 O0, O1, O2, O3;
    unsigned knv;
    const unsigned knoff = WS_KN + head * 256 + tb * 4;
    {
      unsigned so1, so2, ti = ((t + 1) & 15) * 8192, st, conf, sp;
      so2 = kvh + ti;
      asm volatile(
      "v_mov_b64 v[136:137], %[one]\n"
      "v_mov_b64 v[138:139], %[one]\n"
      "v_mov_b64 v[140:141], %[one]\n"
      "v_mov_b64 v[142:143], %[one]\n"
      "buffer_load_dwordx4 v[132:135], v164, %[srd], %[so2] offen sc1\n"
      "s_mov_b64 exec, %[msk]\n"
      "buffer_load_dwordx4 v[136:139], v164, %[srd], %[so2] offen offset:1024 sc1\n"
      "buffer_load_dwordx4 v[140:143], v164, %[srd], %[so2] offen offset:1280 sc1\n"
      "s_mov_b64 exec, -1\n"
      "v_mfma_f32_32x32x16_bf16 v[72:87], v[120:123], v[104:107], 0\n"
      "v_mov_b64 v[148:149], %[one]\n"
      "v_mov_b64 v[150:151], %[one]\n"
      "v_mov_b64 v[152:153], %[one]\n"
      "v_mov_b64 v[154:155], %[one]\n"
      "v_mov_b64 v[8:9], 0\n"
      "v_mov_b64 v[10:11], 0\n"
      "v_mov_b64 v[12:13], 0\n"
      "v_mov_b64 v[14:15], 0\n"
      "v_mov_b64 v[16:17], 0\n"
      "v_mov_b64 v[18:19], 0\n"
      "v_mov_b64 v[20:21], 0\n"
      "v_mov_b64 v[22:23], 0\n"
      "v_mov_b64 v[24:25], 0\n"
      "v_mov_b64 v[26:27], 0\n"
      "v_mov_b64 v[28:29], 0\n"
      "v_mov_b64 v[30:31], 0\n"
      "v_mov_b64 v[32:33], 0\n"
      "v_mov_b64 v[34:35], 0\n"
      "v_mov_b64 v[36:37], 0\n"
      "v_mov_b64 v[38:39], 0\n"
      "v_mov_b64 v[40:41], 0\n"
      "v_mov_b64 v[42:43], 0\n"
      "v_mov_b64 v[44:45], 0\n"
      "v_mov_b64 v[46:47], 0\n"
      "v_mov_b64 v[48:49], 0\n"
      "v_mov_b64 v[50:51], 0\n"
      "v_mov_b64 v[52:53], 0\n"
      "v_mov_b64 v[54:55], 0\n"
      "v_mov_b64 v[56:57], 0\n"
      "v_mov_b64 v[58:59], 0\n"
      "v_mov_b64 v[60:61], 0\n"
      "v_mov_b64 v[62:63], 0\n"
      "v_mov_b64 v[64:65], 0\n"
      "v_mov_b64 v[66:67], 0\n"
      "v_mov_b64 v[68:69], 0\n"
      "v_mov_b64 v[70:71], 0\n"
      "v_mov_b64 v[160:161], 0\n"
      "v_mov_b64 v[162:163], 0\n"
      "v_mov_b32 v165, -1\n"
      "s_mov_b32 %[st], 0\n"
      "s_movk_i32 %[conf], 64\n"
      "GMLOOP%=:\n"
      "v_exp_f32 v72, v72\n"
      "v_exp_f32 v73, v73\n"
      "v_mfma_f32_32x32x16_bf16 v[56:71], v[152:155], v[160:163], v[56:71]\n"
      "s_mov_b32 %[so1], %[so2]\n"
      "s_add_u32 %[ti], %[ti], 0x2000\n"
      "s_and_b32 %[ti], %[ti], 0x1e000\n"
      "s_add_u32 %[so2], %[kvh], %[ti]\n"
      "s_cmp_lt_u32 %[st], 14\n"
      "s_cselect_b32 %[so2], %[so2], 0x7f000000\n"
      "v_exp_f32 v74, v74\n"
      "v_exp_f32 v75, v75\n"
      "v_exp_f32 v76, v76\n"
      "v_exp_f32 v77, v77\n"
      "v_exp_f32 v78, v78\n"
      "v_mfma_f32_32x32x16_bf16 v[88:103], v[120:123], v[108:111], 0\n"
      "buffer_load_dwordx4 v[144:147], v164, %[srd], %[so2] offen sc1\n"
      "s_mov_b64 exec, %[msk]\n"
      "buffer_load_dwordx4 v[148:151], v164, %[srd], %[so2] offen offset:1024 sc1\n"
      "buffer_load_dwordx4 v[152:155], v164, %[srd], %[so2] offen offset:1280 sc1\n"
      "s_mov_b64 exec, -1\n"
      "v_exp_f32 v79, v79\n"
      "v_cvt_pk_bf16_f32 v156, v72, v73\n"
      "v_cvt_pk_bf16_f32 v157, v74, v75\n"
      "v_cvt_pk_bf16_f32 v158, v76, v77\n"
      "v_cvt_pk_bf16_f32 v159, v78, v79\n"
      "v_exp_f32 v80, v80\n"
      "v_exp_f32 v81, v81\n"
      "v_mfma_f32_32x32x16_bf16 v[8:23], v[124:127], v[156:159], v[8:23]\n"
      "v_exp_f32 v82, v82\n"
      "v_exp_f32 v83, v83\n"
      "v_exp_f32 v84, v84\n"
      "v_exp_f32 v85, v85\n"
      "v_exp_f32 v86, v86\n"
      "v_exp_f32 v87, v87\n"
      "v_cvt_pk_bf16_f32 v160, v80, v81\n"
      "v_cvt_pk_bf16_f32 v161, v82, v83\n"
      "v_cvt_pk_bf16_f32 v162, v84, v85\n"
      "v_cvt_pk_bf16_f32 v163, v86, v87\n"
      "v_exp_f32 v88, v88\n"
      "v_exp_f32 v89, v89\n"
      "v_mfma_f32_32x32x16_bf16 v[8:23], v[128:131], v[160:163], v[8:23]\n"
      "v_exp_f32 v90, v90\n"
      "v_exp_f32 v91, v91\n"
      "v_exp_f32 v92, v92\n"
      "v_exp_f32 v93, v93\n"
      "v_exp_f32 v94, v94\n"
      "v_mfma_f32_32x32x16_bf16 v[72:87], v[120:123], v[112:115], 0\n"
      "v_exp_f32 v95, v95\n"
      "v_cvt_pk_bf16_f32 v156, v88, v89\n"
      "v_cvt_pk_bf16_f32 v157, v90, v91\n"
      "v_cvt_pk_bf16_f32 v158, v92, v93\n"
      "v_cvt_pk_bf16_f32 v159, v94, v95\n"
      "v_exp_f32 v96, v96\n"
      "v_exp_f32 v97, v97\n"
      "v_mfma_f32_32x32x16_bf16 v[24:39], v[124:127], v[156:159], v[24:39]\n"
      "v_exp_f32 v98, v98\n"
      "v_exp_f32 v99, v99\n"
      "v_exp_f32 v100, v100\n"
      "v_exp_f32 v101, v101\n"
      "v_exp_f32 v102, v102\n"
      "v_exp_f32 v103, v103\n"
      "v_cvt_pk_bf16_f32 v160, v96, v97\n"
      "v_cvt_pk_bf16_f32 v161, v98, v99\n"
      "v_cvt_pk_bf16_f32 v162, v100, v101\n"
      "v_cvt_pk_bf16_f32 v163, v102, v103\n"
      "v_exp_f32 v72, v72\n"
      "v_exp_f32 v73, v73\n"
      "v_mfma_f32_32x32x16_bf16 v[24:39], v[128:131], v[160:163], v[24:39]\n"
      "v_exp_f32 v74, v74\n"
      "v_exp_f32 v75, v75\n"
      "v_exp_f32 v76, v76\n"
      "v_exp_f32 v77, v77\n"
      "v_exp_f32 v78, v78\n"
      "v_mfma_f32_32x32x16_bf16 v[88:103], v[120:123], v[116:119], 0\n"
      "v_exp_f32 v79, v79\n"
      "v_cvt_pk_bf16_f32 v156, v72, v73\n"
      "v_cvt_pk_bf16_f32 v157, v74, v75\n"
      "v_cvt_pk_bf16_f32 v158, v76, v77\n"
      "v_cvt_pk_bf16_f32 v159, v78, v79\n"
      "v_exp_f32 v80, v80\n"
      "v_exp_f32 v81, v81\n"
      "v_mfma_f32_32x32x16_bf16 v[40:55], v[124:127], v[156:159], v[40:55]\n"
      "s_add_u32 %[st], %[st], 1\n"
      "v_exp_f32 v82, v82\n"
      "v_exp_f32 v83, v83\n"
      "v_exp_f32 v84, v84\n"
      "v_exp_f32 v85, v85\n"
      "v_exp_f32 v86, v86\n"
      "v_exp_f32 v87, v87\n"
      "v_cvt_pk_bf16_f32 v160, v80, v81\n"
      "v_cvt_pk_bf16_f32 v161, v82, v83\n"
      "v_cvt_pk_bf16_f32 v162, v84, v85\n"
      "v_cvt_pk_bf16_f32 v163, v86, v87\n"
      "v_exp_f32 v88, v88\n"
      "v_exp_f32 v89, v89\n"
      "v_mfma_f32_32x32x16_bf16 v[40:55], v[128:131], v[160:163], v[40:55]\n"
      "s_waitcnt vmcnt(3)\n"
      "s_cmp_lt_u32 %[st], %[conf]\n"
      "s_cbranch_scc1 GMCHKA%=\n"
      "GMRETA%=:\n"
      "v_exp_f32 v90, v90\n"
      "v_exp_f32 v91, v91\n"
      "v_exp_f32 v92, v92\n"
      "v_exp_f32 v93, v93\n"
      "v_exp_f32 v94, v94\n"
      "v_mfma_f32_32x32x16_bf16 v[72:87], v[132:135], v[104:107], 0\n"
      "v_exp_f32 v95, v95\n"
      "v_cvt_pk_bf16_f32 v156, v88, v89\n"
      "v_cvt_pk_bf16_f32 v157, v90, v91\n"
      "v_cvt_pk_bf16_f32 v158, v92, v93\n"
      "v_cvt_pk_bf16_f32 v159, v94, v95\n"
      "v_exp_f32 v96, v96\n"
      "v_exp_f32 v97, v97\n"
      "v_mfma_f32_32x32x16_bf16 v[56:71], v[124:127], v[156:159], v[56:71]\n"
      "v_exp_f32 v98, v98\n"
      "v_exp_f32 v99, v99\n"
      "v_exp_f32 v100, v100\n"
      "v_exp_f32 v101, v101\n"
      "v_exp_f32 v102, v102\n"
      "v_exp_f32 v103, v103\n"
      "v_cvt_pk_bf16_f32 v160, v96, v97\n"
      "v_cvt_pk_bf16_f32 v161, v98, v99\n"
      "v_cvt_pk_bf16_f32 v162, v100, v101\n"
      "v_cvt_pk_bf16_f32 v163, v102, v103\n"
      "s_cmp_eq_u32 %[st], 16\n"
      "s_cbranch_scc1 GMEND%=\n"
      "v_exp_f32 v72, v72\n"
      "v_exp_f32 v73, v73\n"
      "v_mfma_f32_32x32x16_bf16 v[56:71], v[128:131], v[160:163], v[56:71]\n"
      "s_mov_b32 %[so1], %[so2]\n"
      "s_add_u32 %[ti], %[ti], 0x2000\n"
      "s_and_b32 %[ti], %[ti], 0x1e000\n"
      "s_add_u32 %[so2], %[kvh], %[ti]\n"
      "s_cmp_lt_u32 %[st], 14\n"
      "s_cselect_b32 %[so2], %[so2], 0x7f000000\n"
      "v_exp_f32 v74, v74\n"
      "v_exp_f32 v75, v75\n"
      "v_exp_f32 v76, v76\n"
      "v_exp_f32 v77, v77\n"
      "v_exp_f32 v78, v78\n"
      "v_mfma_f32_32x32x16_bf16 v[88:103], v[132:135], v[108:111], 0\n"
      "buffer_load_dwordx4 v[120:123], v164, %[srd], %[so2] offen sc1\n"
      "s_mov_b64 exec, %[msk]\n"
      "buffer_load_dwordx4 v[124:127], v164, %[srd], %[so2] offen offset:1024 sc1\n"
      "buffer_load_dwordx4 v[128:131], v164, %[srd], %[so2] offen offset:1280 sc1\n"
      "s_mov_b64 exec, -1\n"
      "v_exp_f32 v79, v79\n"
      "v_cvt_pk_bf16_f32 v156, v72, v73\n"
      "v_cvt_pk_bf16_f32 v157, v74, v75\n"
      "v_cvt_pk_bf16_f32 v158, v76, v77\n"
      "v_cvt_pk_bf16_f32 v159, v78, v79\n"
      "v_exp_f32 v80, v80\n"
      "v_exp_f32 v81, v81\n"
      "v_mfma_f32_32x32x16_bf16 v[8:23], v[136:139], v[156:159], v[8:23]\n"
      "v_exp_f32 v82, v82\n"
      "v_exp_f32 v83, v83\n"
      "v_exp_f32 v84, v84\n"
      "v_exp_f32 v85, v85\n"
      "v_exp_f32 v86, v86\n"
      "v_exp_f32 v87, v87\n"
      "v_cvt_pk_bf16_f32 v160, v80, v81\n"
      "v_cvt_pk_bf16_f32 v161, v82, v83\n"
      "v_cvt_pk_bf16_f32 v162, v84, v85\n"
      "v_cvt_pk_bf16_f32 v163, v86, v87\n"
      "v_exp_f32 v88, v88\n"
      "v_exp_f32 v89, v89\n"
      "v_mfma_f32_32x32x16_bf16 v[8:23], v[140:143], v[160:163], v[8:23]\n"
      "v_exp_f32 v90, v90\n"
      "v_exp_f32 v91, v91\n"
      "v_exp_f32 v92, v92\n"
      "v_exp_f32 v93, v93\n"
      "v_exp_f32 v94, v94\n"
      "v_mfma_f32_32x32x16_bf16 v[72:87], v[132:135], v[112:115], 0\n"
      "v_exp_f32 v95, v95\n"
      "v_cvt_pk_bf16_f32 v156, v88, v89\n"
      "v_cvt_pk_bf16_f32 v157, v90, v91\n"
      "v_cvt_pk_bf16_f32 v158, v92, v93\n"
      "v_cvt_pk_bf16_f32 v159, v94, v95\n"
      "v_exp_f32 v96, v96\n"
      "v_exp_f32 v97, v97\n"
      "v_mfma_f32_32x32x16_bf16 v[24:39], v[136:139], v[156:159], v[24:39]\n"
      "v_exp_f32 v98, v98\n"
      "v_exp_f32 v99, v99\n"
      "v_exp_f32 v100, v100\n"
      "v_exp_f32 v101, v101\n"
      "v_exp_f32 v102, v102\n"
      "v_exp_f32 v103, v103\n"
      "v_cvt_pk_bf16_f32 v160, v96, v97\n"
      "v_cvt_pk_bf16_f32 v161, v98, v99\n"
      "v_cvt_pk_bf16_f32 v162, v100, v101\n"
      "v_cvt_pk_bf16_f32 v163, v102, v103\n"
      "v_exp_f32 v72, v72\n"
      "v_exp_f32 v73, v73\n"
      "v_mfma_f32_32x32x16_bf16 v[24:39], v[140:143], v[160:163], v[24:39]\n"
      "v_exp_f32 v74, v74\n"
      "v_exp_f32 v75, v75\n"
      "v_exp_f32 v76, v76\n"
      "v_exp_f32 v77, v77\n"
      "v_exp_f32 v78, v78\n"
      "v_mfma_f32_32x32x16_bf16 v[88:103], v[132:135], v[116:119], 0\n"
      "v_exp_f32 v79, v79\n"
      "v_cvt_pk_bf16_f32 v156, v72, v73\n"
      "v_cvt_pk_bf16_f32 v157, v74, v75\n"
      "v_cvt_pk_bf16_f32 v158, v76, v77\n"
      "v_cvt_pk_bf16_f32 v159, v78, v79\n"
      "v_exp_f32 v80, v80\n"
      "v_exp_f32 v81, v81\n"
      "v_mfma_f32_32x32x16_bf16 v[40:55], v[136:139], v[156:159], v[40:55]\n"
      "s_add_u32 %[st], %[st], 1\n"
      "v_exp_f32 v82, v82\n"
      "v_exp_f32 v83, v83\n"
      "v_exp_f32 v84, v84\n"
      "v_exp_f32 v85, v85\n"
      "v_exp_f32 v86, v86\n"
      "v_exp_f32 v87, v87\n"
      "v_cvt_pk_bf16_f32 v160, v80, v81\n"
      "v_cvt_pk_bf16_f32 v161, v82, v83\n"
      "v_cvt_pk_bf16_f32 v162, v84, v85\n"
      "v_cvt_pk_bf16_f32 v163, v86, v87\n"
      "v_exp_f32 v88, v88\n"
      "v_exp_f32 v89, v89\n"
      "v_mfma_f32_32x32x16_bf16 v[40:55], v[140:143], v[160:163], v[40:55]\n"
      "s_waitcnt vmcnt(3)\n"
      "s_cmp_lt_u32 %[st], %[conf]\n"
      "s_cbranch_scc1 GMCHKB%=\n"
      "GMRETB%=:\n"
      "v_exp_f32 v90, v90\n"
      "v_exp_f32 v91, v91\n"
      "v_exp_f32 v92, v92\n"
      "v_exp_f32 v93, v93\n"
      "v_exp_f32 v94, v94\n"
      "v_mfma_f32_32x32x16_bf16 v[72:87], v[144:147], v[104:107], 0\n"
      "v_exp_f32 v95, v95\n"
      "v_cvt_pk_bf16_f32 v156, v88, v89\n"
      "v_cvt_pk_bf16_f32 v157, v90, v91\n"
      "v_cvt_pk_bf16_f32 v158, v92, v93\n"
      "v_cvt_pk_bf16_f32 v159, v94, v95\n"
      "v_exp_f32 v96, v96\n"
      "v_exp_f32 v97, v97\n"
      "v_mfma_f32_32x32x16_bf16 v[56:71], v[136:139], v[156:159], v[56:71]\n"
      "v_exp_f32 v98, v98\n"
      "v_exp_f32 v99, v99\n"
      "v_exp_f32 v100, v100\n"
      "v_exp_f32 v101, v101\n"
      "v_exp_f32 v102, v102\n"
      "v_exp_f32 v103, v103\n"
      "v_cvt_pk_bf16_f32 v160, v96, v97\n"
      "v_cvt_pk_bf16_f32 v161, v98, v99\n"
      "v_cvt_pk_bf16_f32 v162, v100, v101\n"
      "v_cvt_pk_bf16_f32 v163, v102, v103\n"
      "v_exp_f32 v72, v72\n"
      "v_exp_f32 v73, v73\n"
      "v_mfma_f32_32x32x16_bf16 v[56:71], v[140:143], v[160:163], v[56:71]\n"
      "s_mov_b32 %[so1], %[so2]\n"
      "s_add_u32 %[ti], %[ti], 0x2000\n"
      "s_and_b32 %[ti], %[ti], 0x1e000\n"
      "s_add_u32 %[so2], %[kvh], %[ti]\n"
      "s_cmp_lt_u32 %[st], 14\n"
      "s_cselect_b32 %[so2], %[so2], 0x7f000000\n"
      "v_exp_f32 v74, v74\n"
      "v_exp_f32 v75, v75\n"
      "v_exp_f32 v76, v76\n"
      "v_exp_f32 v77, v77\n"
      "v_exp_f32 v78, v78\n"
      "v_mfma_f32_32x32x16_bf16 v[88:103], v[144:147], v[108:111], 0\n"
      "buffer_load_dwordx4 v[132:135], v164, %[srd], %[so2] offen sc1\n"
      "s_mov_b64 exec, %[msk]\n"
      "buffer_load_dwordx4 v[136:139], v164, %[srd], %[so2] offen offset:1024 sc1\n"
      "buffer_load_dwordx4 v[140:143], v164, %[srd], %[so2] offen offset:1280 sc1\n"
      "s_mov_b64 exec, -1\n"
      "v_exp_f32 v79, v79\n"
      "v_cvt_pk_bf16_f32 v156, v72, v73\n"
      "v_cvt_pk_bf16_f32 v157, v74, v75\n"
      "v_cvt_pk_bf16_f32 v158, v76, v77\n"
      "v_cvt_pk_bf16_f32 v159, v78, v79\n"
      "v_exp_f32 v80, v80\n"
      "v_exp_f32 v81, v81\n"
      "v_mfma_f32_32x32x16_bf16 v[8:23], v[148:151], v[156:159], v[8:23]\n"
      "v_exp_f32 v82, v82\n"
      "v_exp_f32 v83, v83\n"
      "v_exp_f32 v84, v84\n"
      "v_exp_f32 v85, v85\n"
      "v_exp_f32 v86, v86\n"
      "v_exp_f32 v87, v87\n"
      "v_cvt_pk_bf16_f32 v160, v80, v81\n"
      "v_cvt_pk_bf16_f32 v161, v82, v83\n"
      "v_cvt_pk_bf16_f32 v162, v84, v85\n"
      "v_cvt_pk_bf16_f32 v163, v86, v87\n"
      "v_exp_f32 v88, v88\n"
      "v_exp_f32 v89, v89\n"
      "v_mfma_f32_32x32x16_bf16 v[8:23], v[152:155], v[160:163], v[8:23]\n"
      "v_exp_f32 v90, v90\n"
      "v_exp_f32 v91, v91\n"
      "v_exp_f32 v92, v92\n"
      "v_exp_f32 v93, v93\n"
      "v_exp_f32 v94, v94\n"
      "v_mfma_f32_32x32x16_bf16 v[72:87], v[144:147], v[112:115], 0\n"
      "v_exp_f32 v95, v95\n"
      "v_cvt_pk_bf16_f32 v156, v88, v89\n"
      "v_cvt_pk_bf16_f32 v157, v90, v91\n"
      "v_cvt_pk_bf16_f32 v158, v92, v93\n"
      "v_cvt_pk_bf16_f32 v159, v94, v95\n"
      "v_exp_f32 v96, v96\n"
      "v_exp_f32 v97, v97\n"
      "v_mfma_f32_32x32x16_bf16 v[24:39], v[148:151], v[156:159], v[24:39]\n"
      "v_exp_f32 v98, v98\n"
      "v_exp_f32 v99, v99\n"
      "v_exp_f32 v100, v100\n"
      "v_exp_f32 v101, v101\n"
      "v_exp_f32 v102, v102\n"
      "v_exp_f32 v103, v103\n"
      "v_cvt_pk_bf16_f32 v160, v96, v97\n"
      "v_cvt_pk_bf16_f32 v161, v98, v99\n"
      "v_cvt_pk_bf16_f32 v162, v100, v101\n"
      "v_cvt_pk_bf16_f32 v163, v102, v103\n"
      "v_exp_f32 v72, v72\n"
      "v_exp_f32 v73, v73\n"
      "v_mfma_f32_32x32x16_bf16 v[24:39], v[152:155], v[160:163], v[24:39]\n"
      "v_exp_f32 v74, v74\n"
      "v_exp_f32 v75, v75\n"
      "v_exp_f32 v76, v76\n"
      "v_exp_f32 v77, v77\n"
      "v_exp_f32 v78, v78\n"
      "v_mfma_f32_32x32x16_bf16 v[88:103], v[144:147], v[116:119], 0\n"
      "v_exp_f32 v79, v79\n"
      "v_cvt_pk_bf16_f32 v156, v72, v73\n"
      "v_cvt_pk_bf16_f32 v157, v74, v75\n"
      "v_cvt_pk_bf16_f32 v158, v76, v77\n"
      "v_cvt_pk_bf16_f32 v159, v78, v79\n"
      "v_exp_f32 v80, v80\n"
      "v_exp_f32 v81, v81\n"
      "v_mfma_f32_32x32x16_bf16 v[40:55], v[148:151], v[156:159], v[40:55]\n"
      "s_add_u32 %[st], %[st], 1\n"
      "v_exp_f32 v82, v82\n"
      "v_exp_f32 v83, v83\n"
      "v_exp_f32 v84, v84\n"
      "v_exp_f32 v85, v85\n"
      "v_exp_f32 v86, v86\n"
      "v_exp_f32 v87, v87\n"
      "v_cvt_pk_bf16_f32 v160, v80, v81\n"
      "v_cvt_pk_bf16_f32 v161, v82, v83\n"
      "v_cvt_pk_bf16_f32 v162, v84, v85\n"
      "v_cvt_pk_bf16_f32 v163, v86, v87\n"
      "v_exp_f32 v88, v88\n"
      "v_exp_f32 v89, v89\n"
      "v_mfma_f32_32x32x16_bf16 v[40:55], v[152:155], v[160:163], v[40:55]\n"
      "s_waitcnt vmcnt(3)\n"
      "s_cmp_lt_u32 %[st], %[conf]\n"
      "s_cbranch_scc1 GMCHKC%=\n"
      "GMRETC%=:\n"
      "v_exp_f32 v90, v90\n"
      "v_exp_f32 v91, v91\n"
      "v_exp_f32 v92, v92\n"
      "v_exp_f32 v93, v93\n"
      "v_exp_f32 v94, v94\n"
      "v_mfma_f32_32x32x16_bf16 v[72:87], v[120:123], v[104:107], 0\n"
      "v_exp_f32 v95, v95\n"
      "v_cvt_pk_bf16_f32 v156, v88, v89\n"
      "v_cvt_pk_bf16_f32 v157, v90, v91\n"
      "v_cvt_pk_bf16_f32 v158, v92, v93\n"
      "v_cvt_pk_bf16_f32 v159, v94, v95\n"
      "v_exp_f32 v96, v96\n"
      "v_exp_f32 v97, v97\n"
      "v_mfma_f32_32x32x16_bf16 v[56:71], v[148:151], v[156:159], v[56:71]\n"
      "v_exp_f32 v98, v98\n"
      "v_exp_f32 v99, v99\n"
      "v_exp_f32 v100, v100\n"
      "v_exp_f32 v101, v101\n"
      "v_exp_f32 v102, v102\n"
      "v_exp_f32 v103, v103\n"
      "v_cvt_pk_bf16_f32 v160, v96, v97\n"
      "v_cvt_pk_bf16_f32 v161, v98, v99\n"
      "v_cvt_pk_bf16_f32 v162, v100, v101\n"
      "v_cvt_pk_bf16_f32 v163, v102, v103\n"
      "s_branch GMLOOP%=\n"
      "GMCHKA%=:\n"
      "s_waitcnt vmcnt(0)\n"
      "s_cmp_lg_u32 %[st], 1\n"
      "s_cbranch_scc1 GMNK%=\n"
      "v_mov_b32 v166, %[km]\n"
      "s_mov_b64 exec, 1\n"
      "buffer_store_dword v166, off, %[srd], %[kno] sc1\n"
      "s_mov_b64 exec, -1\n"
      "GMNK%=:\n"
      "s_movk_i32 %[sp], 0x4000\n"
      "GMSPA%=:\n"
      "v_max3_u32 v166, v132, v133, v134\n"
      "v_max3_u32 v166, v166, v135, v136\n"
      "v_max3_u32 v166, v166, v137, v138\n"
      "v_max3_u32 v166, v166, v139, v140\n"
      "v_max3_u32 v166, v166, v141, v142\n"
      "v_max_u32 v166, v166, v143\n"
      "v_cmp_eq_u32 vcc, -1, v166\n"
      "s_cbranch_vccz GMOKA%=\n"
      "s_sub_u32 %[sp], %[sp], 1\n"
      "s_cbranch_scc1 GMOKA%=\n"
      "buffer_load_dwordx4 v[132:135], v164, %[srd], %[so1] offen sc1\n"
      "s_mov_b64 exec, %[msk]\n"
      "buffer_load_dwordx4 v[136:139], v164, %[srd], %[so1] offen offset:1024 sc1\n"
      "buffer_load_dwordx4 v[140:143], v164, %[srd], %[so1] offen offset:1280 sc1\n"
      "s_mov_b64 exec, -1\n"
      "s_waitcnt vmcnt(0)\n"
      "s_branch GMSPA%=\n"
      "GMOKA%=:\n"
      "v_max3_u32 v166, v144, v145, v146\n"
      "v_max3_u32 v166, v166, v147, v148\n"
      "v_max3_u32 v166, v166, v149, v150\n"
      "v_max3_u32 v166, v166, v151, v152\n"
      "v_max3_u32 v166, v166, v153, v154\n"
      "v_max_u32 v166, v166, v155\n"
      "v_cmp_eq_u32 vcc, -1, v166\n"
      "s_cbranch_vccz GMPA%=\n"
      "buffer_load_dwordx4 v[144:147], v164, %[srd], %[so2] offen sc1\n"
      "s_mov_b64 exec, %[msk]\n"
      "buffer_load_dwordx4 v[148:151], v164, %[srd], %[so2] offen offset:1024 sc1\n"
      "buffer_load_dwordx4 v[152:155], v164, %[srd], %[so2] offen offset:1280 sc1\n"
      "s_mov_b64 exec, -1\n"
      "GMPA%=:\n"
      "v_cmp_eq_u32 vcc, -1, v165\n"
      "s_cbranch_vccz GMCFA%=\n"
      "buffer_load_dword v165, v167, %[srd], %[knb] offen sc1\n"
      "s_branch GMRETA%=\n"
      "GMCFA%=:\n"
      "s_add_u32 %[sp], %[st], 2\n"
      "s_min_u32 %[conf], %[conf], %[sp]\n"
      "s_branch GMRETA%=\n"
      "GMCHKB%=:\n"
      "s_waitcnt vmcnt(0)\n"
      "s_movk_i32 %[sp], 0x4000\n"
      "GMSPB%=:\n"
      "v_max3_u32 v166, v144, v145, v146\n"
      "v_max3_u32 v166, v166, v147, v148\n"
      "v_max3_u32 v166, v166, v149, v150\n"
      "v_max3_u32 v166, v166, v151, v152\n"
      "v_max3_u32 v166, v166, v153, v154\n"
      "v_max_u32 v166, v166, v155\n"
      "v_cmp_eq_u32 vcc, -1, v166\n"
      "s_cbranch_vccz GMOKB%=\n"
      "s_sub_u32 %[sp], %[sp], 1\n"
      "s_cbranch_scc1 GMOKB%=\n"
      "buffer_load_dwordx4 v[144:147], v164, %[srd], %[so1] offen sc1\n"
      "s_mov_b64 exec, %[msk]\n"
      "buffer_load_dwordx4 v[148:151], v164, %[srd], %[so1] offen offset:1024 sc1\n"
      "buffer_load_dwordx4 v[152:155], v164, %[srd], %[so1] offen offset:1280 sc1\n"
      "s_mov_b64 exec, -1\n"
      "s_waitcnt vmcnt(0)\n"
      "s_branch GMSPB%=\n"
      "GMOKB%=:\n"
      "v_max3_u32 v166, v120, v121, v122\n"
      "v_max3_u32 v166, v166, v123, v124\n"
      "v_max3_u32 v166, v166, v125, v126\n"
      "v_max3_u32 v166, v166, v127, v128\n"
      "v_max3_u32 v166, v166, v129, v130\n"
      "v_max_u32 v166, v166, v131\n"
      "v_cmp_eq_u32 vcc, -1, v166\n"
      "s_cbranch_vccz GMPB%=\n"
      "buffer_load_dwordx4 v[120:123], v164, %[srd], %[so2] offen sc1\n"
      "s_mov_b64 exec, %[msk]\n"
      "buffer_load_dwordx4 v[124:127], v164, %[srd], %[so2] offen offset:1024 sc1\n"
      "buffer_load_dwordx4 v[128:131], v164, %[srd], %[so2] offen offset:1280 sc1\n"
      "s_mov_b64 exec, -1\n"
      "GMPB%=:\n"
      "v_cmp_eq_u32 vcc, -1, v165\n"
      "s_cbranch_vccz GMCFB%=\n"
      "buffer_load_dword v165, v167, %[srd], %[knb] offen sc1\n"
      "s_branch GMRETB%=\n"
      "GMCFB%=:\n"
      "s_add_u32 %[sp], %[st], 2\n"
      "s_min_u32 %[conf], %[conf], %[sp]\n"
      "s_branch GMRETB%=\n"
      "GMCHKC%=:\n"
      "s_waitcnt vmcnt(0)\n"
      "s_movk_i32 %[sp], 0x4000\n"
      "GMSPC%=:\n"
      "v_max3_u32 v166, v120, v121, v122\n"
      "v_max3_u32 v166, v166, v123, v124\n"
      "v_max3_u32 v166, v166, v125, v126\n"
      "v_max3_u32 v166, v166, v127, v128\n"
      "v_max3_u32 v166, v166, v129, v130\n"
      "v_max_u32 v166, v166, v131\n"
      "v_cmp_eq_u32 vcc, -1, v166\n"
      "s_cbranch_vccz GMOKC%=\n"
      "s_sub_u32 %[sp], %[sp], 1\n"
      "s_cbranch_scc1 GMOKC%=\n"
      "buffer_load_dwordx4 v[120:123], v164, %[srd], %[so1] offen sc1\n"
      "s_mov_b64 exec, %[msk]\n"
      "buffer_load_dwordx4 v[124:127], v164, %[srd], %[so1] offen offset:1024 sc1\n"
      "buffer_load_dwordx4 v[128:131], v164, %[srd], %[so1] offen offset:1280 sc1\n"
      "s_mov_b64 exec, -1\n"
      "s_waitcnt vmcnt(0)\n"
      "s_branch GMSPC%=\n"
      "GMOKC%=:\n"
      "v_max3_u32 v166, v132, v133, v134\n"
      "v_max3_u32 v166, v166, v135, v136\n"
      "v_max3_u32 v166, v166, v137, v138\n"
      "v_max3_u32 v166, v166, v139, v140\n"
      "v_max3_u32 v166, v166, v141, v142\n"
      "v_max_u32 v166, v166, v143\n"
      "v_cmp_eq_u32 vcc, -1, v166\n"
      "s_cbranch_vccz GMPC%=\n"
      "buffer_load_dwordx4 v[132:135], v164, %[srd], %[so2] offen sc1\n"
      "s_mov_b64 exec, %[msk]\n"
      "buffer_load_dwordx4 v[136:139], v164, %[srd], %[so2] offen offset:1024 sc1\n"
      "buffer_load_dwordx4 v[140:143], v164, %[srd], %[so2] offen offset:1280 sc1\n"
      "s_mov_b64 exec, -1\n"
      "GMPC%=:\n"
      "v_cmp_eq_u32 vcc, -1, v165\n"
      "s_cbranch_vccz GMCFC%=\n"
      "buffer_load_dword v165, v167, %[srd], %[knb] offen sc1\n"
      "s_branch GMRETC%=\n"
      "GMCFC%=:\n"
      "s_add_u32 %[sp], %[st], 2\n"
      "s_min_u32 %[conf], %[conf], %[sp]\n"
      "s_branch GMRETC%=\n"
      "GMEND%=:\n"
      "s_nop 1\n"
      "v_mfma_f32_32x32x16_bf16 v[56:71], v[128:131], v[160:163], v[56:71]\n"
      "s_waitcnt vmcnt(0)\n"
      "s_nop 15\n"
          : "={v[8:23]}"(O0), "={v[24:39]}"(O1), "={v[40:55]}"(O2), "={v[56:71]}"(O3), "+{v[120:123]}"(KA), "+{v[124:127]}"(VA0),
            "+{v[128:131]}"(VA1), "={v165}"(knv), [so1] "=&s"(so1), [so2] "+s"(so2), [ti] "+s"(ti), [st] "=&s"(st),
            [conf] "=&s"(conf), [sp] "=&s"(sp)
          : "{v[104:107]}"(Q0), "{v[108:111]}"(Q1), "{v[112:115]}"(Q2), "{v[116:119]}"(Q3), "{v164}"(l16), "{v167}"((lane & 15) * 16),
            [srd] "s"(sws), [msk] "s"(0x0000ffff0000ffffull), [one] "s"(0x3f803f803f803f80ull), [kvh] "s"(kvh), [km] "s"(km),
            [kno] "s"(knoff + t * 16), [knb] "s"(knoff)
          : "memory", "scc", "vcc",
            "v72", "v73", "v74", "v75", "v76", "v77", "v78", "v79", "v80", "v81", "v82", "v83", "v84", "v85", "v86", "v87", "v88", "v89", "v90", "v91", "v92", "v93", "v94", "v95", "v96", "v97", "v98", "v99", "v100", "v101", "v102", "v103", "v132", "v133", "v134", "v135", "v136", "v137", "v138", "v139", "v140", "v141", "v142", "v143", "v144", "v145", "v146", "v147", "v148", "v149", "v150", "v151", "v152", "v153", "v154", "v155", "v156", "v157", "v158", "v159", "v160", "v161", "v162", "v163", "v166");
    }
    {
    const int lane = fresh_lane(), r = lane & 31, hh = lane >> 5;
    const unsigned l16 = lane * 16;
    larrive(M_ADONE + hd * 4);
#pragma unroll 1
    for (int i = 0; i < SPIN_MAX && __any(knv == 0xffffffffu); ++i) knv = ld4c_wait(sws, (lane & 15) * 16, knoff);
    float m0 = 0.f, m1 = 0.f, m2 = 0.f, m3 = 0.f;
    {
      const float qm = *lp<float>(M_QMAX + hd * 4);
      const bool safe = __all(qm * __builtin_bit_cast(float, knv) <= SAFE2);
      if (!safe) {
        if (lane == 0) *lp<unsigned>(M_SLOW + hd * 4) = 1u;
        m0 = m1 = m2 = m3 = -3.0e38f;
#pragma unroll
        for (int i = 0; i < 16; ++i) { O0[i] = 0.f; O1[i] = 0.f; O2[i] = 0.f; O3[i] = 0.f; }
        const f32x16 z = {0, 0, 0, 0, 0, 0, 0, 0, 0, 0, 0, 0, 0, 0, 0, 0};
#pragma unroll 1
        for (int ss = 0; ss < 16; ++ss) {
          kv_load(KA, VA0, VA1, sws, l16, kvh + ss * 8192);
          asm volatile("s_waitcnt vmcnt(0)" : "+v"(KA), "+v"(VA0), "+v"(VA1)::"memory");
#define SLOW(Q, O, m)                                                                 \
  {                                                                                   \
    f32x16 S = MFMA(KA, Q, z);                                                        \
    float tm = S[0];                                                                  \
    _Pragma("unroll") for (int i = 1; i < 16; ++i) tm = fmaxf(tm, S[i]);              \
    tm = fmaxf(tm, xhalf(tm, lane));                                                        \
    const float mn = fmaxf(m, tm), al = __builtin_amdgcn_exp2f(m - mn);               \
    m = mn;                                                                           \
    _Pragma("unroll") for (int i = 0; i < 16; ++i) { O[i] *= al; S[i] = __builtin_amdgcn_exp2f(S[i] - mn); } \
    O = MFMA(VA0, pack8<0>(S), O);                                                    \
    O = MFMA(VA1, pack8<8>(S), O);                                                    \
  }
          SLOW(Q0, O0, m0) SLOW(Q1, O1, m1) SLOW(Q2, O2, m2) SLOW(Q3, O3, m3)
        }
      }
    }
    lwait<2>(M_MSYNC, 4);
    {
      const unsigned rb = L_R + hd * 27648;
#define RWR(O, m, j)                                                                                              \
  {                                                                                                               \
    const unsigned a = rb + (((tb + j) & 3) * 3 + (j - 1)) * 2304;                                                \
    f32x4 lo = {O[0], O[1], O[2], O[3]}, hi = {O[4], O[5], O[6], O[7]};                                           \
    *lp<f32x4>(a + l16) = lo;                                                                                \
    *lp<f32x4>(a + 1024 + l16) = hi;                                                                         \
    *lp<float>(a + 2048 + r * 8 + hh * 4) = hh ? m : O[8];                                                   \
  }
      RWR(O1, m1, 1) RWR(O2, m2, 2) RWR(O3, m3, 3)
      larrive(M_RSYNC + hd * 4);
      lwait<0>(M_RSYNC + hd * 4, 4);
      f32x4 lo[3], hi[3];
      f32x2 lm[3];
#pragma unroll
      for (int j = 0; j < 3; ++j) {
        const unsigned a = rb + (tb * 3 + j) * 2304;
        lo[j] = *lp<f32x4>(a + l16);
        hi[j] = *lp<f32x4>(a + 1024 + l16);
        lm[j] = *lp<f32x2>(a + 2048 + r * 8);
      }
      float l = O0[8];
      if (lword(M_SLOW + hd * 4)) {
        const float mm = fmaxf(fmaxf(m0, lm[0][1]), fmaxf(lm[1][1], lm[2][1]));
        const float a0 = __builtin_amdgcn_exp2f(m0 - mm);
        l *= a0;
#pragma unroll
        for (int i = 0; i < 8; ++i) O0[i] *= a0;
#pragma unroll
        for (int j = 0; j < 3; ++j) {
          const float aj = __builtin_amdgcn_exp2f(lm[j][1] - mm);
          lo[j] *= aj; hi[j] *= aj; lm[j][0] *= aj;
        }
      }
#pragma unroll
      for (int j = 0; j < 3; ++j) {
        l += lm[j][0];
#pragma unroll
        for (int i = 0; i < 4; ++i) { O0[i] += lo[j][i]; O0[4 + i] += hi[j][i]; }
      }
      const float il = __builtin_amdgcn_rcpf(l);
#pragma unroll
      for (int i = 0; i < 8; ++i) O0[i] *= il;
      st16c(nosent4(pack8<0>(O0)), sws, l16, WS_O + ((4 * t + tb) * 32 + head) * 1024);
    }
    }
    return;
  }

  const int m = w - 8;
  if (m < 3) {
    const u32x4 sw = make_srd(wqkv, 512 * 1536 * 2);
    u32x4 wr[32];
    const unsigned vo = (lane >> 2) * 3072 + (lane & 3) * 16;
#pragma unroll
    for (int j = 0; j < 32; ++j) ld16(wr[j], sw, vo, j * 16 * 3072 + m * 1024 + p * 64);
    const unsigned hsel = (lane >> 1) & 1;
    const unsigned da = L_WR + (lane >> 2) * 64 + (m < 2 ? hsel * 4096 + ((m ^ hsel) * 32) + (lane & 1) * 16 : 8192 + (lane & 3) * 16);
#pragma unroll
    for (int c = 0; c < 8; ++c) {
      asm volatile("s_waitcnt vmcnt(%4)" : "+v"(wr[4 * c]), "+v"(wr[4 * c + 1]), "+v"(wr[4 * c + 2]), "+v"(wr[4 * c + 3]) : "i"(28 - 4 * c) : "memory");
#pragma unroll
      for (int j = 0; j < 4; ++j) lwr16(da + (c & 1) * 12288 + j * 1024, wr[4 * c + j]);
      bar();
    }
  } else {
    unsigned ev;
    ld4c(ev, sws, 0, WS_E);
    const u32x4 sent = {0xffffffffu, 0xffffffffu, 0xffffffffu, 0xffffffffu};
#pragma unroll
    for (int j = 0; j < 16; ++j) st16c(sent, sws, l16, WS_KV + ((2 * p + (j >> 3)) * 64 + 4 * t) * 2048 + (j & 7) * 1024);
#pragma unroll
    for (int j = 0; j < 8; ++j) st16c(sent, sws, l16, WS_O + ((4 * t + (j >> 1)) * 32 + 2 * p + (j & 1)) * 1024);
    if (lane < 2) st16c(sent, sws, lane * 256, WS_KN + 2 * p * 256 + t * 16);
    if (lane < 32) *lp<unsigned>(L_MISC + lane * 4) = 0u;
    asm volatile("s_waitcnt vmcnt(0)" : "+v"(ev)::"memory");
    const unsigned tg = ev + 1u;
    if (lane == 0) { st4c(tg, sws, 0, WS_ARM + (p * 16 + t) * 4); *lp<unsigned>(M_TAG) = tg; }
#pragma unroll
    for (int c = 0; c < 7; ++c) bar();
    const unsigned fo = lane < 16 ? (p * 16 + lane) * 4 : ((lane & 15) * 16 + (rt >> 1)) * 4;
#pragma unroll 1
    for (int i = 0; i < SPIN_MAX; ++i) {
      if (__all(ld4c_wait(sws, fo, WS_ARM) == tg)) break;
      __builtin_amdgcn_s_sleep(2);
    }
    bar();
  }
  bar();
  bar();
  const unsigned tag = lword(M_TAG);
  const unsigned trn = (8 * hh + tq) * 64 + tblk * 32 + tp * 8;
  if (p < 14) {
    const u32x4 s1 = make_srd(w1, 512 * 1792 * 2);
    const unsigned ring = L_W1 + m * 7168;
    const unsigned vo = (lane >> 2) * 3584 + (lane & 3) * 16;
    const unsigned so = (128 * p + 32 * m) * 2;
#pragma unroll
    for (int j = 0; j < 6; ++j) dma16(s1, vo, so + j * 16 * 3584, ring + j * 1024);
    f32x16 a0, a1, a2, a3;
    {
      const u32x2* bp = (const u32x2*)(b1 + 128 * p + 32 * m + 4 * hh);
#pragma unroll
      for (int q = 0; q < 4; ++q) {
        u32x2 v = bp[2 * q];
        a0[4 * q] = bflo(v[0]); a0[4 * q + 1] = bfhi(v[0]); a0[4 * q + 2] = bflo(v[1]); a0[4 * q + 3] = bfhi(v[1]);
      }
      a1 = a0; a2 = a0; a3 = a0;
    }
    int rs = 0, wsl = 6;
#pragma unroll 1
    for (int c = 0; c < 8; ++c) {
#pragma unroll
      for (int s = 0; s < 4; ++s) {
        const int ks = 4 * c + s;
        asm volatile("s_waitcnt vmcnt(5) lgkmcnt(0)" ::: "memory");
        dma16(s1, vo, ks + 6 < 32 ? so + (ks + 6) * 16 * 3584 : OOB, ring + wsl * 1024);
        u32x4 fw = ltr(ring + rs * 1024 + trn, 256);
        const unsigned xa = L_X + (xbase ^ (s << 5)) + c * 16384;
        u32x4 x0 = lrd16(xa), x1 = lrd16(xa + 4096), x2 = lrd16(xa + 8192), x3 = lrd16(xa + 12288);
        a0 = MFMA(fw, x0, a0); a1 = MFMA(fw, x1, a1); a2 = MFMA(fw, x2, a2); a3 = MFMA(fw, x3, a3);
        SB;
        rs = rs == 6 ? 0 : rs + 1;
        wsl = wsl == 6 ? 0 : wsl + 1;
      }
    }
    larrive(M_MSYNC);
#define HOUT(a, tbb)                                                                              \
  {                                                                                               \
    _Pragma("unroll") for (int i = 0; i < 16; ++i) { float v = fmaxf(a[i], 0.f); a[i] = v * v; }  \
    const unsigned o = WS_H + ((4 * t + tbb) * 112 + 8 * p + 2 * m) * 1024;                       \
    st16c(pack8<0>(a), sws, l16, o);                                                              \
    st16c(pack8<8>(a), sws, l16, o + 1024);                                                       \
  }
    HOUT(a0, 0) HOUT(a1, 1) HOUT(a2, 2) HOUT(a3, 3)
    VMCNT(0);
    if (lane == 0) st4c(tag, sws, 0, WS_HF + (t * 64 + 4 * p + m) * 4);
  } else if (p == 14) {
    f32x16 a;
#pragma unroll
    for (int i = 0; i < 16; ++i) a[i] = 0.f;
#pragma unroll 1
    for (int c = 0; c < 8; ++c) {
#pragma unroll
      for (int s = 0; s < 4; ++s) {
        u32x4 gf = *(const u32x4*)(gw + (4 * c + s) * 16 + hh * 8);
        u32x4 xf = lrd16(L_X + (xbase ^ (s << 5)) + c * 16384 + m * 4096);
        a = MFMA(gf, xf, a);
      }
    }
    larrive(M_MSYNC);
    const float g = __builtin_amdgcn_rcpf(1.f + __builtin_amdgcn_exp2f(-1.4426950408889634f * a[0]));
    if (lane < 32) st4c(__builtin_bit_cast(unsigned, g), sws, lane * 4, WS_G + (128 * t + 32 * m) * 4);
    VMCNT(0);
    if (lane == 0) st4c(tag, sws, 0, WS_HF + (t * 64 + 56 + m) * 4);
  } else {
    larrive(M_MSYNC);
  }
  lwait<2>(M_MSYNC, 4);

  const int rb = m >> 1, cb = m & 1;
  const unsigned dvo = (lane >> 3) * 1024 + (((lane & 7) ^ (((lane >> 4) & 1) << 2)) << 4);
  const unsigned trp0 = (4 * hh + tq) * 128 + ((cb ^ (tq >> 1)) << 6) + tblk * 32 + tp * 8;
  f32x16 c0, c1, c2, c3;
  {
    const u32x4 s2 = make_srd(w2, 1792 * 512 * 2);
    const unsigned ring = L_W2 + m * 16384;
    const unsigned so = 28 * m * 16 * 1024 + ct * 128;
#pragma unroll
    for (int j = 0; j < 6; ++j) {
      dma16(s2, dvo, so + j * 16384, ring + j * 2048);
      dma16(s2, dvo, so + j * 16384 + 8192, ring + j * 2048 + 1024);
    }
    {
      const unsigned fo = (lane < 14 ? 14 * m + lane : 56 + 2 * (rt & 1) + rb) * 4;
  #pragma unroll 1
    for (int i = 0; i < SPIN_MAX; ++i) {
        if (__all(ld4c_wait(sws, fo, WS_HF + (rt >> 1) * 256) == tag)) break;
        __builtin_amdgcn_s_sleep(8);
      }
    }
    const unsigned h0 = WS_H + ((2 * rt + rb) * 112 + 28 * m) * 1024, h1 = WS_H + ((2 * rt + (rb ^ 1)) * 112 + 28 * m) * 1024;
    u32x4 ha[7], hb[7];
#pragma unroll
    for (int j = 0; j < 6; ++j) { ld16c(ha[j], sws, l16, h0 + j * 1024); ld16c(hb[j], sws, l16, h1 + j * 1024); }
#pragma unroll
    for (int i = 0; i < 16; ++i) { c0[i] = 0.f; c1[i] = 0.f; c2[i] = 0.f; c3[i] = 0.f; }
#pragma unroll 1
    for (int it = 0; it < 4; ++it) {
#pragma unroll
      for (int j = 0; j < 7; ++j) {
        const int ks = it * 7 + j, jn = (j + 6) % 7;
        if (j < 6 && it == 0) asm volatile("s_waitcnt vmcnt(%2)" : "+v"(ha[j]), "+v"(hb[j]) : "i"(10 + 2 * j) : "memory");
        asm volatile("s_waitcnt vmcnt(20) lgkmcnt(0)" : "+v"(ha[j]), "+v"(hb[j])::"memory");
        const bool more = ks + 6 < 28;
        dma16(s2, dvo, more ? so + (ks + 6) * 16384 : OOB, ring + jn * 2048);
        dma16(s2, dvo, more ? so + (ks + 6) * 16384 + 8192 : OOB, ring + jn * 2048 + 1024);
        ld16c(ha[jn], sws, l16, more ? h0 + (ks + 6) * 1024 : OOB);
        ld16c(hb[jn], sws, l16, more ? h1 + (ks + 6) * 1024 : OOB);
        u32x4 f0 = ltr(ring + j * 2048 + trp0, 1024);
        u32x4 f1 = ltr(ring + j * 2048 + (trp0 ^ 64), 1024);
        c0 = MFMA(f0, ha[j], c0); c1 = MFMA(f1, ha[j], c1); c2 = MFMA(f0, hb[j], c2); c3 = MFMA(f1, hb[j], c3);
        SB;
      }
    }
    asm volatile("s_waitcnt vmcnt(0)" : "+v"(ha[0]), "+v"(ha[1]), "+v"(ha[2]), "+v"(ha[3]), "+v"(ha[4]), "+v"(ha[5]), "+v"(hb[0]), "+v"(hb[1]), "+v"(hb[2]), "+v"(hb[3]), "+v"(hb[4]), "+v"(hb[5])::"memory");
#define BWR(c, i)                                                                                  \
  _Pragma("unroll") for (int q = 0; q < 4; ++q) {                                                  \
    f32x4 v = {c[4 * q], c[4 * q + 1], c[4 * q + 2], c[4 * q + 3]};                                \
    *lp<f32x4>(ring + (i - 1) * 4096 + q * 1024 + l16) = v;                                   \
  }
    BWR(c1, 1) BWR(c2, 2) BWR(c3, 3)
    larrive(M_MSYNC);
    lwait<1>(M_MSYNC, 8);
#pragma unroll
    for (int i = 1; i < 4; ++i)
#pragma unroll
      for (int q = 0; q < 4; ++q) {
        f32x4 v = *lp<f32x4>(L_W2 + (m ^ i) * 16384 + (i - 1) * 4096 + q * 1024 + l16);
        c0[4 * q] += v[0]; c0[4 * q + 1] += v[1]; c0[4 * q + 2] += v[2]; c0[4 * q + 3] += v[3];
      }
    larrive(M_MSYNC);
    lwait<1>(M_MSYNC, 12);
  }
  {
    const u32x4 so_ = make_srd(wo, 512 * 512 * 2);
#pragma unroll
    for (int j = 0; j < 16; ++j) dma16(so_, dvo, (128 * m + 8 * j) * 1024 + ct * 128, L_W2 + m * 16384 + j * 1024);
  }
  const float g = __builtin_bit_cast(float, ld4c_wait(sws, r * 4, WS_G + (64 * rt + 32 * rb) * 4));
  larrive(M_MSYNC);
  {
    const u32x2* bp = (const u32x2*)(b2 + 64 * ct + 32 * cb + 4 * hh);
    const float og = 1.f - g;
#pragma unroll
    for (int q = 0; q < 4; ++q) {
      u32x2 v = bp[2 * q];
      c0[4 * q] = og * (c0[4 * q] + bflo(v[0])); c0[4 * q + 1] = og * (c0[4 * q + 1] + bfhi(v[0]));
      c0[4 * q + 2] = og * (c0[4 * q + 2] + bflo(v[1])); c0[4 * q + 3] = og * (c0[4 * q + 3] + bfhi(v[1]));
    }
  }
  lwait<1>(M_MSYNC, 16);
#pragma unroll
  for (int i = 0; i < 16; ++i) c1[i] = 0.f;
#pragma unroll 1
  for (int par = 0; par < 2; ++par) {
    lwait<4>(M_ADONE + par * 4, 1);
    u32x4 of[16];
    const unsigned ob = WS_O + ((2 * rt + rb) * 32 + par) * 1024;
#pragma unroll 1
    for (int i = 0; i < SPIN_MAX; ++i) {
#pragma unroll
      for (int j = 0; j < 16; ++j) ld16c(of[j], sws, l16, ob + j * 2048);
      asm volatile("s_waitcnt vmcnt(0)"
                   : "+v"(of[0]), "+v"(of[1]), "+v"(of[2]), "+v"(of[3]), "+v"(of[4]), "+v"(of[5]), "+v"(of[6]), "+v"(of[7]),
                     "+v"(of[8]), "+v"(of[9]), "+v"(of[10]), "+v"(of[11]), "+v"(of[12]), "+v"(of[13]), "+v"(of[14]), "+v"(of[15])::"memory");
      unsigned mx = 0;
#pragma unroll
      for (int j = 0; j < 16; ++j) mx = umax4(of[j], mx);
      if (!__any(mx == 0xffffffffu)) break;
    }
#pragma unroll
    for (int j = 0; j < 16; ++j) {
      u32x4 f = ltr(L_W2 + par * 2048 + j * 4096 + trp0, 1024);
      c1 = MFMA(f, of[j], c1);
    }
  }
  {
    unsigned o[8];
#pragma unroll
    for (int i = 0; i < 8; ++i) o[i] = pk2(g * c1[2 * i] + c0[2 * i], g * c1[2 * i + 1] + c0[2 * i + 1]);
    const u32x4 sout = make_srd(out, 2048 * 512 * 2);
    const unsigned vo = r * 1024 + hh * 16;
    const unsigned so = (64 * rt + 32 * rb) * 1024 + (64 * ct + 32 * cb) * 2;
#pragma unroll
    for (int k = 0; k < 2; ++k) {
      auto rx = __builtin_amdgcn_permlane32_swap(o[4 * k], o[4 * k + 2], false, false);
      auto ry = __builtin_amdgcn_permlane32_swap(o[4 * k + 1], o[4 * k + 3], false, false);
      u32x4 v = {rx[0], ry[0], rx[1], ry[1]};
      st16(v, sout, vo + k * 32, so);
    }
  }
  if (blockIdx.x == 0 && m == 0 && lane == 0) st4c(tag, sws, 0, WS_E);
}

extern "C" void kernel_launch(void* const* d_in, const int* in_sizes, int n_in, void* d_out, int out_size, void* d_ws,
                              size_t ws_size, hipStream_t stream) {
  gm_kernel<<<256, 768, 0, stream>>>((const u16*)d_in[0], (const u16*)d_in[1], (const u16*)d_in[2], (const u16*)d_in[4],
                                     (const u16*)d_in[5], (const u16*)d_in[6], (const u16*)d_in[7], (const u16*)d_in[8],
                                     (u16*)d_out, d_ws);
}
```

```cpp
#include <hip/hip_runtime.h>
#include <stdint.h>

typedef __attribute__((ext_vector_type(8))) __bf16 bf16x8;
typedef __attribute__((ext_vector_type(4))) __bf16 bf16x4;
typedef __attribute__((ext_vector_type(2))) __bf16 bf16x2;
typedef __attribute__((ext_vector_type(2))) float f32x2;
typedef __attribute__((ext_vector_type(4))) float f32x4;
typedef __attribute__((ext_vector_type(16))) float f32x16;
typedef __attribute__((ext_vector_type(4))) unsigned u32x4;
typedef __attribute__((ext_vector_type(2))) unsigned u32x2;
typedef unsigned short u16;
#define LAS __attribute__((address_space(3)))
#define DI __device__ __forceinline__
#define SB __builtin_amdgcn_sched_barrier(0)
#define MFMA(a, b, c) __builtin_amdgcn_mfma_f32_32x32x16_bf16(__builtin_bit_cast(bf16x8, a), __builtin_bit_cast(bf16x8, b), (c), 0, 0, 0)

constexpr unsigned WS_E = 0, WS_G = 8192, WS_ARM = 0x8000, WS_HF = 0x10000, WS_FL = 0x40000, WS_DBG = 0x80000;
constexpr unsigned WS_KV = 1u << 20, WS_O = 9u << 20, WS_H = 11u << 20, WS_BYTES = 18u << 20;
constexpr unsigned OOB = 0x7f000000u;
constexpr unsigned L_X = 0, L_WR = 131072, L_QX = 131072, L_W1 = 131072, L_MISC = 159744;
constexpr unsigned L_VX = L_WR + 8192, L_R = 0, L_W2 = 65536;
constexpr unsigned M_OARM = L_MISC, M_MSYNC = L_MISC + 4, M_ADONE = L_MISC + 8, M_RSYNC = L_MISC + 16, M_SLOW = L_MISC + 24,
                   M_QRD = L_MISC + 32;
#define HSTEPS 32
#define BSTEPS 28
#define VSPLIT 1
#define HPRIO 0
#define BPRIO 0
#define LOOK 1
constexpr int SPIN_MAX = 1 << 14;
constexpr float SAFE2 = 40.f * 40.f;

DI u32x4 make_srd(const void* p, unsigned bytes) {
  uint64_t a = (uint64_t)p;
  u32x4 r = {(unsigned)a, (unsigned)(a >> 32) & 0xffffu, bytes, 0x00020000u};
  return r;
}
DI void dma16(u32x4 srd, unsigned voff, unsigned soff, unsigned ldsaddr) {
  asm volatile("s_mov_b32 m0, %3\n\ts_nop 0\n\tbuffer_load_dwordx4 %0, %1, %2 offen lds" ::"v"(voff), "s"(srd), "s"(soff), "s"(ldsaddr) : "memory");
}
DI void ld16(u32x4& d, u32x4 srd, unsigned voff, unsigned soff) {
  asm volatile("buffer_load_dwordx4 %0, %1, %2, %3 offen" : "=v"(d) : "v"(voff), "s"(srd), "s"(soff) : "memory");
}
DI void ld16c(u32x4& d, u32x4 srd, unsigned voff, unsigned soff) {
  asm volatile("buffer_load_dwordx4 %0, %1, %2, %3 offen sc1" : "=v"(d) : "v"(voff), "s"(srd), "s"(soff) : "memory");
}
DI void ld4c(unsigned& d, u32x4 srd, unsigned voff, unsigned soff) {
  asm volatile("buffer_load_dword %0, %1, %2, %3 offen sc1" : "=v"(d) : "v"(voff), "s"(srd), "s"(soff) : "memory");
}
DI unsigned ld4c_wait(u32x4 srd, unsigned voff, unsigned soff) {
  unsigned d;
  asm volatile("buffer_load_dword %0, %1, %2, %3 offen sc1\n\ts_waitcnt vmcnt(0)" : "=&v"(d) : "v"(voff), "s"(srd), "s"(soff) : "memory");
  return d;
}
DI void st16c(u32x4 v, u32x4 srd, unsigned voff, unsigned soff) {
  asm volatile("buffer_store_dwordx4 %0, %1, %2, %3 offen sc1\n\ts_nop 1" ::"v"(v), "v"(voff), "s"(srd), "s"(soff) : "memory");
}
DI void st16(u32x4 v, u32x4 srd, unsigned voff, unsigned soff) {
  asm volatile("buffer_store_dwordx4 %0, %1, %2, %3 offen\n\ts_nop 1" ::"v"(v), "v"(voff), "s"(srd), "s"(soff) : "memory");
}
DI void st4c(unsigned v, u32x4 srd, unsigned voff, unsigned soff) {
  asm volatile("buffer_store_dword %0, %1, %2, %3 offen sc1" ::"v"(v), "v"(voff), "s"(srd), "s"(soff) : "memory");
}
#define VMCNT(n) asm volatile("s_waitcnt vmcnt(" #n ")" ::: "memory")
#define LGKM0 asm volatile("s_waitcnt lgkmcnt(0)" ::: "memory")
DI void bar() { asm volatile("s_waitcnt lgkmcnt(0)\n\ts_barrier" ::: "memory"); }

DI float asf(unsigned v) { return __builtin_bit_cast(float, v); }
DI unsigned asu(float v) { return __builtin_bit_cast(unsigned, v); }
DI bf16x2 asb(unsigned v) { return __builtin_bit_cast(bf16x2, v); }
DI unsigned pk2(float a, float b) { f32x2 v = {a, b}; return __builtin_bit_cast(unsigned, __builtin_convertvector(v, bf16x2)); }
DI unsigned nosent(unsigned v) { return v < 0xfffffffeu ? v : 0xfffffffeu; }
template <int O> DI u32x4 pack8(const f32x16& a) {
  u32x4 r = {pk2(a[O], a[O + 1]), pk2(a[O + 2], a[O + 3]), pk2(a[O + 4], a[O + 5]), pk2(a[O + 6], a[O + 7])};
  return r;
}
DI u32x4 nosent4(u32x4 v) { u32x4 r = {nosent(v[0]), nosent(v[1]), nosent(v[2]), nosent(v[3])}; return r; }
DI unsigned umax3(unsigned a, unsigned b, unsigned c) { unsigned m = a > b ? a : b; return m > c ? m : c; }
DI unsigned umax4(u32x4 v, unsigned m) { return umax3(umax3(m, v[0], v[1]), v[2], v[3]); }
DI float bflo(unsigned v) { return asf(v << 16); }
DI float bfhi(unsigned v) { return asf(v & 0xffff0000u); }
DI int fresh_lane() { int l; asm volatile("v_mbcnt_lo_u32_b32 %0, -1, 0\n\tv_mbcnt_hi_u32_b32 %0, -1, %0" : "=v"(l)); return l; }
DI float xhalf(float v, int lane) {
  unsigned u = asu(v);
  auto r = __builtin_amdgcn_permlane32_swap(u, u, false, false);
  return asf((lane & 32) ? r[0] : r[1]);
}

template <int C, int RM> DI unsigned dppmax(unsigned v) {
  unsigned o = __builtin_amdgcn_update_dpp(0u, v, C, RM, 0xf, false);
  return v > o ? v : o;
}
DI unsigned half_umax(unsigned v) {
  v = dppmax<0x111, 0xf>(v); v = dppmax<0x112, 0xf>(v); v = dppmax<0x114, 0xf>(v); v = dppmax<0x118, 0xf>(v);
  return dppmax<0x142, 0xa>(v);
}
DI float sumsq(u32x4 v, float c) {
#pragma unroll
  for (int i = 0; i < 4; ++i) c = __builtin_amdgcn_fdot2_f32_bf16(asb(v[i]), asb(v[i]), c, false);
  return c;
}
#define PRIO(n) __builtin_amdgcn_s_setprio(n)

DI unsigned load_tag(const void* ws) {
  unsigned e;
  asm volatile("s_load_dword %0, %1, 0x0\n\ts_waitcnt lgkmcnt(0)" : "=s"(e) : "s"(ws) : "memory");
  return e + 1u;
}
__shared__ __attribute__((aligned(16))) char lds[163840];
template <class T> DI T* lp(unsigned a) { return (T*)(lds + a); }
DI u32x4 lrd16(unsigned a) { return *lp<u32x4>(a); }
DI void lwr16(unsigned a, u32x4 v) { *lp<u32x4>(a) = v; }
DI u32x4 ltr(unsigned a, unsigned d) {
  bf16x4 lo = __builtin_amdgcn_ds_read_tr16_b64_v4bf16((LAS bf16x4*)(lds + a));
  bf16x4 hi = __builtin_amdgcn_ds_read_tr16_b64_v4bf16((LAS bf16x4*)(lds + a + d));
  return __builtin_bit_cast(u32x4, __builtin_shufflevector(lo, hi, 0, 1, 2, 3, 4, 5, 6, 7));
}
DI unsigned lword(unsigned a) { return __builtin_amdgcn_readfirstlane(__hip_atomic_load(lp<unsigned>(a), __ATOMIC_RELAXED, __HIP_MEMORY_SCOPE_WORKGROUP)); }
DI void larrive(unsigned a) {
  asm volatile("s_waitcnt lgkmcnt(0)\n\ts_mov_b64 exec, 1\n\tds_add_u32 %0, %1\n\ts_mov_b64 exec, -1" ::"v"(a), "v"(1u) : "memory");
}
template <int SLP> DI void lwait(unsigned a, unsigned target) {
#pragma unroll 1
  for (int i = 0; i < (SPIN_MAX << 4); ++i) {
    if (lword(a) >= target) break;
    __builtin_amdgcn_s_sleep(SLP);
  }
  asm volatile("" ::: "memory");
}

DI void kv_load(u32x4& K, u32x4& V0, u32x4& V1, u32x4 srd, unsigned voff, unsigned soff) {
  asm volatile(
      "buffer_load_dwordx4 %0, %3, %4, %5 offen sc1\n\t"
      "s_mov_b64 exec, %6\n\t"
      "buffer_load_dwordx4 %1, %3, %4, %5 offen offset:1024 sc1\n\t"
      "buffer_load_dwordx4 %2, %3, %4, %5 offen offset:1280 sc1\n\t"
      "s_mov_b64 exec, -1"
      : "+v"(K), "+v"(V0), "+v"(V1)
      : "v"(voff), "s"(srd), "s"(soff), "s"(0x0000ffff0000ffffull)
      : "memory");
}
__global__ void __launch_bounds__(768, 3)
gm_kernel(const u16* x, const u16* wqkv, const u16* wo, const u16* w1, const u16* b1, const u16* w2, const u16* b2,
          const u16* gw, u16* out, void* ws) {
  const int w = __builtin_amdgcn_readfirstlane(threadIdx.x >> 6);
  const int xcd = blockIdx.x & 7, idx = blockIdx.x >> 3;
  const int t = (xcd & 1) * 8 + (idx & 7), p = (xcd >> 1) * 4 + (idx >> 3);
#define ROT (t + 2 * p)
  const int kc0 = ROT;
  const u32x4 sws = make_srd(ws, WS_BYTES);
  asm volatile("s_nop 4");

  if (w >= 4) {
    const int lane = fresh_lane(), r = lane & 31, hh = lane >> 5;
    const unsigned l16 = lane * 16;
    const unsigned xbase = r * 128 + ((hh ^ ((r >> 1) & 7)) << 4);
    const int tq = (lane & 15) >> 2, tp = lane & 3, tblk = (lane >> 4) & 1;
    const int a = w - 4, tb = a & 3, hd = a >> 2, head = 2 * p + hd;
    const unsigned kvh = WS_KV + head * 0x40000 + tb * 2048;
    PRIO(1);
    const unsigned tag = load_tag(ws);
    if (a == 0 && lane < 32) *lp<unsigned>(L_MISC + lane * 4) = 0u;
    const u32x4 sent = {0xffffffffu, 0xffffffffu, 0xffffffffu, 0xffffffffu};
    unsigned armv = 0;
    f32x16 aqk, av;
#pragma unroll
    for (int i = 0; i < 16; ++i) { aqk[i] = 0.f; av[i] = 0.f; }
    {
      const unsigned wb = (8 * hh + tq) * 64 + (tblk ^ hd) * 32 + tp * 8;
      unsigned wq[2] = {L_WR + wb + hd * 5120, L_WR + wb + hd * 3072}, wv[2] = {L_WR + wb + 8192 + hd * 1024, L_WR + wb + 8192 - hd * 1024};
      unsigned xa[2][4];
#pragma unroll
      for (int s = 0; s < 4; ++s) { xa[0][s] = (L_X + tb * 4096 + xbase) ^ ((s ^ hd) << 5); xa[1][s] = xa[0][s] + 65536; asm volatile("" : "+v"(xa[0][s]), "+v"(xa[1][s])); }
      asm volatile("" : "+v"(wq[0]), "+v"(wq[1]), "+v"(wv[0]), "+v"(wv[1]));
      u32x4 xf[2][4], fq[2][4], fv[2][4];
#pragma unroll
      for (int c = 0; c < 9; ++c) {
        if (c == 5) VMCNT(0);
        if (c < 8 + VSPLIT) bar();
#define PRD(s) if (c < 8) { xf[c & 1][s] = lrd16(xa[c >> 2][s] + (c & 3) * 16384); fq[c & 1][s] = ltr(wq[s & 1] + (c & 1) * 12288 + s * 1024, 256); }
#define PRV(s) if (c < 8 && !(VSPLIT && (s & 1))) fv[c & 1][s] = ltr(wv[s & 1] + (c & 1) * 12288 + s * 1024, 256);
#define PMQ(s) SB; if (c > 0) aqk = MFMA(fq[(c - 1) & 1][s], xf[(c - 1) & 1][s], aqk); SB;
#define PMV(s) SB; if (c > 0 && !(VSPLIT && (s & 1))) av = MFMA(xf[(c - 1) & 1][s], fv[(c - 1) & 1][s], av); SB;
        PMV(0) PRV(0) PRD(0) PMQ(0) PRD(1) PRV(1) PMV(1) PRV(2) PMV(2) PRD(2) PMQ(1) PRD(3) PRV(3) PMV(3)
        if (c == 0) { st16c(sent, sws, l16, kvh + t * 8192); st16c(sent, sws, l16, kvh + t * 8192 + 1024); }
        if (c == 5 && a == 0 && lane < 32) st4c(tag, sws, lane * 4, WS_ARM + (p * 16 + t) * 128);
        if (c == 8 && a == 0) ld4c(armv, sws, (lane & 15) * 128, WS_ARM + p * 2048);
        PMQ(2) PMQ(3)
      }
    }
    u32x4 KA, VA0, VA1, Q0, Q1, Q2, Q3;
    unsigned km;
    float qm;
    const unsigned pad = (r & 27) == 16 ? 0x3f803f80u : 0u;
    const u32x4 ones = {pad, pad, pad, pad};
    {
#define VOUT()                                              \
  VA0 = nosent4(pack8<0>(av));                              \
  VA1 = nosent4(pack8<8>(av));                              \
  if (r < 16) {                                             \
    st16c(VA0, sws, l16 + 1024, kvh + t * 8192);            \
    st16c(VA1, sws, l16 + 1280, kvh + t * 8192);            \
  } else {                                                  \
    VA0 = ones; VA1 = ones;                                 \
  }
      const unsigned vx = (hh * 16 + (r & 15)) * 16;
      if (VSPLIT) {
        if (r >= 16) {
#pragma unroll
          for (int q = 0; q < 4; ++q) { f32x4 v = {av[4 * q], av[4 * q + 1], av[4 * q + 2], av[4 * q + 3]}; *lp<f32x4>(L_VX + a * 2048 + q * 512 + vx) = v; }
        }
      } else { VOUT() }
      SB;
      KA = nosent4(pack8<8>(aqk));
      st16c(KA, sws, l16, kvh + t * 8192);
      SB;
      const float qs = 0.25f * 1.4426950408889634f;
#pragma unroll
      for (int i = 0; i < 8; ++i) aqk[i] *= qs;
      Q0 = pack8<0>(aqk);
      lwr16(L_QX + a * 1024 + l16, Q0);
      if (a == 0) {
        asm volatile("s_waitcnt vmcnt(%1)" : "+v"(armv) : "i"(VSPLIT ? 1 : 3) : "memory");
#pragma unroll 1
        for (int i = 0; i < SPIN_MAX && !__all(armv == tag); ++i) armv = ld4c_wait(sws, (lane & 15) * 128, WS_ARM + p * 2048);
      }
      bar();
      if (VSPLIT) {
        f32x4 pv[4];
#pragma unroll
        for (int q = 0; q < 4; ++q) pv[q] = *lp<f32x4>(L_VX + (a ^ 4) * 2048 + q * 512 + vx);
#pragma unroll
        for (int i = 0; i < 16; ++i) av[i] += pv[i >> 2][i & 3];
        VOUT()
      }
      Q1 = lrd16(L_QX + hd * 4096 + ((tb + 1) & 3) * 1024 + l16);
      Q2 = lrd16(L_QX + hd * 4096 + ((tb + 2) & 3) * 1024 + l16);
      Q3 = lrd16(L_QX + hd * 4096 + ((tb + 3) & 3) * 1024 + l16);
      SB;
      const float q0n = sumsq(Q0, 0.f), kn = sumsq(KA, 0.f), v0n = sumsq(VA0, 0.f), v1n = sumsq(VA1, 0.f);
      SB;
      larrive(M_QRD);
      const u32x4 qn4 = {asu(q0n), asu(sumsq(Q1, 0.f)), asu(sumsq(Q2, 0.f)), asu(sumsq(Q3, 0.f))};
      auto sw = __builtin_amdgcn_permlane32_swap(umax4(qn4, 0u), asu(kn), false, false);
      const unsigned nm = half_umax(asu(asf(sw[0]) + asf(sw[1])));
      qm = asf(__builtin_amdgcn_readlane(nm, 31));
      km = __builtin_amdgcn_readlane(nm, 63);
      if (__any(!(v0n + v1n <= 1e24f))) km = 0x7f800000u;
    }

    f32x16 O0, O1, O2, O3;
    u32x2 fl = {pad, pad};
    const unsigned flb = WS_FL + (head * 4 + tb) * 2048;
    {
      unsigned so = kvh + t * 8192, s1, st, conf, sp;
      asm volatile(
      "s_nop 1\n"
      "v_mfma_f32_32x32x16_bf16 v[72:87], v[120:123], v[104:107], 0\n"
      "v_mov_b64 v[136:137], v[166:167]\n"
      "v_mov_b64 v[138:139], v[166:167]\n"
      "v_mov_b64 v[140:141], v[166:167]\n"
      "v_mov_b64 v[142:143], v[166:167]\n"
      "v_mov_b64 v[148:149], v[166:167]\n"
      "v_mov_b64 v[150:151], v[166:167]\n"
      "v_mov_b64 v[152:153], v[166:167]\n"
      "v_mov_b64 v[154:155], v[166:167]\n"
      "s_mov_b32 %[st], 1\n"
      "s_movk_i32 %[conf], 64\n"
      "s_nop 1\n"
      "v_exp_f32 v72, v72\n"
      "v_exp_f32 v73, v73\n"
      "v_exp_f32 v74, v74\n"
      "v_exp_f32 v75, v75\n"
      "v_exp_f32 v76, v76\n"
      "v_exp_f32 v77, v77\n"
      "v_exp_f32 v78, v78\n"
      "v_mfma_f32_32x32x16_bf16 v[88:103], v[120:123], v[108:111], 0\n"
      "v_exp_f32 v79, v79\n"
      "v_cvt_pk_bf16_f32 v156, v72, v73\n"
      "v_cvt_pk_bf16_f32 v157, v74, v75\n"
      "v_cvt_pk_bf16_f32 v158, v76, v77\n"
      "v_cvt_pk_bf16_f32 v159, v78, v79\n"
      "v_exp_f32 v80, v80\n"
      "v_exp_f32 v81, v81\n"
      "v_mfma_f32_32x32x16_bf16 v[8:23], v[124:127], v[156:159], 0\n"
      "v_exp_f32 v82, v82\n"
      "v_exp_f32 v83, v83\n"
      "v_exp_f32 v84, v84\n"
      "v_exp_f32 v85, v85\n"
      "v_exp_f32 v86, v86\n"
      "v_exp_f32 v87, v87\n"
      "v_cvt_pk_bf16_f32 v160, v80, v81\n"
      "v_cvt_pk_bf16_f32 v161, v82, v83\n"
      "v_cvt_pk_bf16_f32 v162, v84, v85\n"
      "v_cvt_pk_bf16_f32 v163, v86, v87\n"
      "v_exp_f32 v88, v88\n"
      "v_exp_f32 v89, v89\n"
      "v_mfma_f32_32x32x16_bf16 v[8:23], v[128:131], v[160:163], v[8:23]\n"
      "v_mov_b32 v156, %[km]\n"
      "v_mov_b32 v157, %[tag]\n"
      "v_mov_b32 v158, %[km]\n"
      "v_mov_b32 v159, %[tag]\n"
      "s_waitcnt vmcnt(0)\n"
      "s_add_u32 %[so], %[so], 0x2000\n"
      "s_bitset0_b32 %[so], 17\n"
      "buffer_load_dwordx4 v[132:135], v164, %[srd], %[so] offen sc1\n"
      "s_mov_b64 exec, %[msk]\n"
      "buffer_load_dwordx4 v[136:139], v164, %[srd], %[so] offen offset:1024 sc1\n"
      "buffer_load_dwordx4 v[140:143], v164, %[srd], %[so] offen offset:1280 sc1\n"
      "s_mov_b64 exec, -1\n"
      "s_mov_b64 exec, 0xff\n"
      "buffer_store_dwordx4 v[156:159], v164, %[srd], %[flo] offen sc1\n"
      "s_mov_b64 exec, -1\n"
      "v_exp_f32 v90, v90\n"
      "v_exp_f32 v91, v91\n"
      "v_exp_f32 v92, v92\n"
      "v_exp_f32 v93, v93\n"
      "v_exp_f32 v94, v94\n"
      "v_mfma_f32_32x32x16_bf16 v[72:87], v[120:123], v[112:115], 0\n"
      "v_exp_f32 v95, v95\n"
      "v_cvt_pk_bf16_f32 v156, v88, v89\n"
      "v_cvt_pk_bf16_f32 v157, v90, v91\n"
      "v_cvt_pk_bf16_f32 v158, v92, v93\n"
      "v_cvt_pk_bf16_f32 v159, v94, v95\n"
      "v_exp_f32 v96, v96\n"
      "v_exp_f32 v97, v97\n"
      "v_mfma_f32_32x32x16_bf16 v[24:39], v[124:127], v[156:159], 0\n"
      "v_exp_f32 v98, v98\n"
      "v_exp_f32 v99, v99\n"
      "v_exp_f32 v100, v100\n"
      "v_exp_f32 v101, v101\n"
      "v_exp_f32 v102, v102\n"
      "v_exp_f32 v103, v103\n"
      "v_cvt_pk_bf16_f32 v160, v96, v97\n"
      "v_cvt_pk_bf16_f32 v161, v98, v99\n"
      "v_cvt_pk_bf16_f32 v162, v100, v101\n"
      "v_cvt_pk_bf16_f32 v163, v102, v103\n"
      "v_exp_f32 v72, v72\n"
      "v_exp_f32 v73, v73\n"
      "v_mfma_f32_32x32x16_bf16 v[24:39], v[128:131], v[160:163], v[24:39]\n"
      "s_add_u32 %[so], %[so], 0x2000\n"
      "s_bitset0_b32 %[so], 17\n"
      "buffer_load_dwordx4 v[144:147], v164, %[srd], %[so] offen sc1\n"
      "s_mov_b64 exec, %[msk]\n"
      "buffer_load_dwordx4 v[148:151], v164, %[srd], %[so] offen offset:1024 sc1\n"
      "buffer_load_dwordx4 v[152:155], v164, %[srd], %[so] offen offset:1280 sc1\n"
      "s_mov_b64 exec, -1\n"
      "v_exp_f32 v74, v74\n"
      "v_exp_f32 v75, v75\n"
      "v_exp_f32 v76, v76\n"
      "v_exp_f32 v77, v77\n"
      "v_exp_f32 v78, v78\n"
      "v_mfma_f32_32x32x16_bf16 v[88:103], v[120:123], v[116:119], 0\n"
      "v_exp_f32 v79, v79\n"
      "v_cvt_pk_bf16_f32 v156, v72, v73\n"
      "v_cvt_pk_bf16_f32 v157, v74, v75\n"
      "v_cvt_pk_bf16_f32 v158, v76, v77\n"
      "v_cvt_pk_bf16_f32 v159, v78, v79\n"
      "v_exp_f32 v80, v80\n"
      "v_exp_f32 v81, v81\n"
      "v_mfma_f32_32x32x16_bf16 v[40:55], v[124:127], v[156:159], 0\n"
      "v_exp_f32 v82, v82\n"
      "v_exp_f32 v83, v83\n"
      "v_exp_f32 v84, v84\n"
      "v_exp_f32 v85, v85\n"
      "v_exp_f32 v86, v86\n"
      "v_exp_f32 v87, v87\n"
      "v_cvt_pk_bf16_f32 v160, v80, v81\n"
      "v_cvt_pk_bf16_f32 v161, v82, v83\n"
      "v_cvt_pk_bf16_f32 v162, v84, v85\n"
      "v_cvt_pk_bf16_f32 v163, v86, v87\n"
      "v_exp_f32 v88, v88\n"
      "v_exp_f32 v89, v89\n"
      "v_mfma_f32_32x32x16_bf16 v[40:55], v[128:131], v[160:163], v[40:55]\n"
      "s_cmp_lt_u32 %[st], %[conf]\n"
      "s_cbranch_scc1 GMCZ%=\n"
      "s_waitcnt vmcnt(3)\n"
      "GMRZ%=:\n"
      "v_exp_f32 v90, v90\n"
      "v_exp_f32 v91, v91\n"
      "v_exp_f32 v92, v92\n"
      "v_exp_f32 v93, v93\n"
      "v_exp_f32 v94, v94\n"
      "v_mfma_f32_32x32x16_bf16 v[72:87], v[132:135], v[104:107], 0\n"
      "v_exp_f32 v95, v95\n"
      "v_cvt_pk_bf16_f32 v156, v88, v89\n"
      "v_cvt_pk_bf16_f32 v157, v90, v91\n"
      "v_cvt_pk_bf16_f32 v158, v92, v93\n"
      "v_cvt_pk_bf16_f32 v159, v94, v95\n"
      "v_exp_f32 v96, v96\n"
      "v_exp_f32 v97, v97\n"
      "v_mfma_f32_32x32x16_bf16 v[56:71], v[124:127], v[156:159], 0\n"
      "v_exp_f32 v98, v98\n"
      "v_exp_f32 v99, v99\n"
      "v_exp_f32 v100, v100\n"
      "v_exp_f32 v101, v101\n"
      "v_exp_f32 v102, v102\n"
      "v_exp_f32 v103, v103\n"
      "v_cvt_pk_bf16_f32 v160, v96, v97\n"
      "v_cvt_pk_bf16_f32 v161, v98, v99\n"
      "v_cvt_pk_bf16_f32 v162, v100, v101\n"
      "v_cvt_pk_bf16_f32 v163, v102, v103\n"
      "GMLOOP%=:\n"
      "v_exp_f32 v72, v72\n"
      "v_exp_f32 v73, v73\n"
      "v_mfma_f32_32x32x16_bf16 v[56:71], v[128:131], v[160:163], v[56:71]\n"
      "s_add_u32 %[so], %[so], 0x2000\n"
      "s_bitset0_b32 %[so], 17\n"
      "v_exp_f32 v74, v74\n"
      "v_exp_f32 v75, v75\n"
      "v_exp_f32 v76, v76\n"
      "v_exp_f32 v77, v77\n"
      "v_exp_f32 v78, v78\n"
      "v_mfma_f32_32x32x16_bf16 v[88:103], v[132:135], v[108:111], 0\n"
      "buffer_load_dwordx4 v[120:123], v164, %[srd], %[so] offen sc1\n"
      "s_mov_b64 exec, %[msk]\n"
      "buffer_load_dwordx4 v[124:127], v164, %[srd], %[so] offen offset:1024 sc1\n"
      "buffer_load_dwordx4 v[128:131], v164, %[srd], %[so] offen offset:1280 sc1\n"
      "s_mov_b64 exec, -1\n"
      "v_exp_f32 v79, v79\n"
      "v_cvt_pk_bf16_f32 v156, v72, v73\n"
      "v_cvt_pk_bf16_f32 v157, v74, v75\n"
      "v_cvt_pk_bf16_f32 v158, v76, v77\n"
      "v_cvt_pk_bf16_f32 v159, v78, v79\n"
      "v_exp_f32 v80, v80\n"
      "v_exp_f32 v81, v81\n"
      "v_mfma_f32_32x32x16_bf16 v[8:23], v[136:139], v[156:159], v[8:23]\n"
      "v_exp_f32 v82, v82\n"
      "v_exp_f32 v83, v83\n"
      "v_exp_f32 v84, v84\n"
      "v_exp_f32 v85, v85\n"
      "v_exp_f32 v86, v86\n"
      "v_exp_f32 v87, v87\n"
      "v_cvt_pk_bf16_f32 v160, v80, v81\n"
      "v_cvt_pk_bf16_f32 v161, v82, v83\n"
      "v_cvt_pk_bf16_f32 v162, v84, v85\n"
      "v_cvt_pk_bf16_f32 v163, v86, v87\n"
      "v_exp_f32 v88, v88\n"
      "v_exp_f32 v89, v89\n"
      "v_mfma_f32_32x32x16_bf16 v[8:23], v[140:143], v[160:163], v[8:23]\n"
      "v_exp_f32 v90, v90\n"
      "v_exp_f32 v91, v91\n"
      "v_exp_f32 v92, v92\n"
      "v_exp_f32 v93, v93\n"
      "v_exp_f32 v94, v94\n"
      "v_mfma_f32_32x32x16_bf16 v[72:87], v[132:135], v[112:115], 0\n"
      "v_exp_f32 v95, v95\n"
      "v_cvt_pk_bf16_f32 v156, v88, v89\n"
      "v_cvt_pk_bf16_f32 v157, v90, v91\n"
      "v_cvt_pk_bf16_f32 v158, v92, v93\n"
      "v_cvt_pk_bf16_f32 v159, v94, v95\n"
      "v_exp_f32 v96, v96\n"
      "v_exp_f32 v97, v97\n"
      "v_mfma_f32_32x32x16_bf16 v[24:39], v[136:139], v[156:159], v[24:39]\n"
      "v_exp_f32 v98, v98\n"
      "v_exp_f32 v99, v99\n"
      "v_exp_f32 v100, v100\n"
      "v_exp_f32 v101, v101\n"
      "v_exp_f32 v102, v102\n"
      "v_exp_f32 v103, v103\n"
      "v_cvt_pk_bf16_f32 v160, v96, v97\n"
      "v_cvt_pk_bf16_f32 v161, v98, v99\n"
      "v_cvt_pk_bf16_f32 v162, v100, v101\n"
      "v_cvt_pk_bf16_f32 v163, v102, v103\n"
      "v_exp_f32 v72, v72\n"
      "v_exp_f32 v73, v73\n"
      "v_mfma_f32_32x32x16_bf16 v[24:39], v[140:143], v[160:163], v[24:39]\n"
      "v_exp_f32 v74, v74\n"
      "v_exp_f32 v75, v75\n"
      "v_exp_f32 v76, v76\n"
      "v_exp_f32 v77, v77\n"
      "v_exp_f32 v78, v78\n"
      "v_mfma_f32_32x32x16_bf16 v[88:103], v[132:135], v[116:119], 0\n"
      "v_exp_f32 v79, v79\n"
      "v_cvt_pk_bf16_f32 v156, v72, v73\n"
      "v_cvt_pk_bf16_f32 v157, v74, v75\n"
      "v_cvt_pk_bf16_f32 v158, v76, v77\n"
      "v_cvt_pk_bf16_f32 v159, v78, v79\n"
      "v_exp_f32 v80, v80\n"
      "v_exp_f32 v81, v81\n"
      "v_mfma_f32_32x32x16_bf16 v[40:55], v[136:139], v[156:159], v[40:55]\n"
      "s_add_u32 %[st], %[st], 1\n"
      "v_exp_f32 v82, v82\n"
      "v_exp_f32 v83, v83\n"
      "v_exp_f32 v84, v84\n"
      "v_exp_f32 v85, v85\n"
      "v_exp_f32 v86, v86\n"
      "v_exp_f32 v87, v87\n"
      "v_cvt_pk_bf16_f32 v160, v80, v81\n"
      "v_cvt_pk_bf16_f32 v161, v82, v83\n"
      "v_cvt_pk_bf16_f32 v162, v84, v85\n"
      "v_cvt_pk_bf16_f32 v163, v86, v87\n"
      "v_exp_f32 v88, v88\n"
      "v_exp_f32 v89, v89\n"
      "v_mfma_f32_32x32x16_bf16 v[40:55], v[140:143], v[160:163], v[40:55]\n"
      "s_cmp_lt_u32 %[st], %[conf]\n"
      "s_cbranch_scc1 GMCB%=\n"
      "s_waitcnt vmcnt(3)\n"
      "GMRB%=:\n"
      "v_exp_f32 v90, v90\n"
      "v_exp_f32 v91, v91\n"
      "v_exp_f32 v92, v92\n"
      "v_exp_f32 v93, v93\n"
      "v_exp_f32 v94, v94\n"
      "v_mfma_f32_32x32x16_bf16 v[72:87], v[144:147], v[104:107], 0\n"
      "v_exp_f32 v95, v95\n"
      "v_cvt_pk_bf16_f32 v156, v88, v89\n"
      "v_cvt_pk_bf16_f32 v157, v90, v91\n"
      "v_cvt_pk_bf16_f32 v158, v92, v93\n"
      "v_cvt_pk_bf16_f32 v159, v94, v95\n"
      "v_exp_f32 v96, v96\n"
      "v_exp_f32 v97, v97\n"
      "v_mfma_f32_32x32x16_bf16 v[56:71], v[136:139], v[156:159], v[56:71]\n"
      "v_exp_f32 v98, v98\n"
      "v_exp_f32 v99, v99\n"
      "v_exp_f32 v100, v100\n"
      "v_exp_f32 v101, v101\n"
      "v_exp_f32 v102, v102\n"
      "v_exp_f32 v103, v103\n"
      "v_cvt_pk_bf16_f32 v160, v96, v97\n"
      "v_cvt_pk_bf16_f32 v161, v98, v99\n"
      "v_cvt_pk_bf16_f32 v162, v100, v101\n"
      "v_cvt_pk_bf16_f32 v163, v102, v103\n"
      "v_exp_f32 v72, v72\n"
      "v_exp_f32 v73, v73\n"
      "v_mfma_f32_32x32x16_bf16 v[56:71], v[140:143], v[160:163], v[56:71]\n"
      "s_add_u32 %[so], %[so], 0x2000\n"
      "s_bitset0_b32 %[so], 17\n"
      "v_exp_f32 v74, v74\n"
      "v_exp_f32 v75, v75\n"
      "v_exp_f32 v76, v76\n"
      "v_exp_f32 v77, v77\n"
      "v_exp_f32 v78, v78\n"
      "v_mfma_f32_32x32x16_bf16 v[88:103], v[144:147], v[108:111], 0\n"
      "buffer_load_dwordx4 v[132:135], v164, %[srd], %[so] offen sc1\n"
      "s_mov_b64 exec, %[msk]\n"
      "buffer_load_dwordx4 v[136:139], v164, %[srd], %[so] offen offset:1024 sc1\n"
      "buffer_load_dwordx4 v[140:143], v164, %[srd], %[so] offen offset:1280 sc1\n"
      "s_mov_b64 exec, -1\n"
      "v_exp_f32 v79, v79\n"
      "v_cvt_pk_bf16_f32 v156, v72, v73\n"
      "v_cvt_pk_bf16_f32 v157, v74, v75\n"
      "v_cvt_pk_bf16_f32 v158, v76, v77\n"
      "v_cvt_pk_bf16_f32 v159, v78, v79\n"
      "v_exp_f32 v80, v80\n"
      "v_exp_f32 v81, v81\n"
      "v_mfma_f32_32x32x16_bf16 v[8:23], v[148:151], v[156:159], v[8:23]\n"
      "v_exp_f32 v82, v82\n"
      "v_exp_f32 v83, v83\n"
      "v_exp_f32 v84, v84\n"
      "v_exp_f32 v85, v85\n"
      "v_exp_f32 v86, v86\n"
      "v_exp_f32 v87, v87\n"
      "v_cvt_pk_bf16_f32 v160, v80, v81\n"
      "v_cvt_pk_bf16_f32 v161, v82, v83\n"
      "v_cvt_pk_bf16_f32 v162, v84, v85\n"
      "v_cvt_pk_bf16_f32 v163, v86, v87\n"
      "v_exp_f32 v88, v88\n"
      "v_exp_f32 v89, v89\n"
      "v_mfma_f32_32x32x16_bf16 v[8:23], v[152:155], v[160:163], v[8:23]\n"
      "v_exp_f32 v90, v90\n"
      "v_exp_f32 v91, v91\n"
      "v_exp_f32 v92, v92\n"
      "v_exp_f32 v93, v93\n"
      "v_exp_f32 v94, v94\n"
      "v_mfma_f32_32x32x16_bf16 v[72:87], v[144:147], v[112:115], 0\n"
      "v_exp_f32 v95, v95\n"
      "v_cvt_pk_bf16_f32 v156, v88, v89\n"
      "v_cvt_pk_bf16_f32 v157, v90, v91\n"
      "v_cvt_pk_bf16_f32 v158, v92, v93\n"
      "v_cvt_pk_bf16_f32 v159, v94, v95\n"
      "v_exp_f32 v96, v96\n"
      "v_exp_f32 v97, v97\n"
      "v_mfma_f32_32x32x16_bf16 v[24:39], v[148:151], v[156:159], v[24:39]\n"
      "v_exp_f32 v98, v98\n"
      "v_exp_f32 v99, v99\n"
      "v_exp_f32 v100, v100\n"
      "v_exp_f32 v101, v101\n"
      "v_exp_f32 v102, v102\n"
      "v_exp_f32 v103, v103\n"
      "v_cvt_pk_bf16_f32 v160, v96, v97\n"
      "v_cvt_pk_bf16_f32 v161, v98, v99\n"
      "v_cvt_pk_bf16_f32 v162, v100, v101\n"
      "v_cvt_pk_bf16_f32 v163, v102, v103\n"
      "v_exp_f32 v72, v72\n"
      "v_exp_f32 v73, v73\n"
      "v_mfma_f32_32x32x16_bf16 v[24:39], v[152:155], v[160:163], v[24:39]\n"
      "v_exp_f32 v74, v74\n"
      "v_exp_f32 v75, v75\n"
      "v_exp_f32 v76, v76\n"
      "v_exp_f32 v77, v77\n"
      "v_exp_f32 v78, v78\n"
      "v_mfma_f32_32x32x16_bf16 v[88:103], v[144:147], v[116:119], 0\n"
      "v_exp_f32 v79, v79\n"
      "v_cvt_pk_bf16_f32 v156, v72, v73\n"
      "v_cvt_pk_bf16_f32 v157, v74, v75\n"
      "v_cvt_pk_bf16_f32 v158, v76, v77\n"
      "v_cvt_pk_bf16_f32 v159, v78, v79\n"
      "v_exp_f32 v80, v80\n"
      "v_exp_f32 v81, v81\n"
      "v_mfma_f32_32x32x16_bf16 v[40:55], v[148:151], v[156:159], v[40:55]\n"
      "s_add_u32 %[st], %[st], 1\n"
      "v_exp_f32 v82, v82\n"
      "v_exp_f32 v83, v83\n"
      "v_exp_f32 v84, v84\n"
      "v_exp_f32 v85, v85\n"
      "v_exp_f32 v86, v86\n"
      "v_exp_f32 v87, v87\n"
      "v_cvt_pk_bf16_f32 v160, v80, v81\n"
      "v_cvt_pk_bf16_f32 v161, v82, v83\n"
      "v_cvt_pk_bf16_f32 v162, v84, v85\n"
      "v_cvt_pk_bf16_f32 v163, v86, v87\n"
      "v_exp_f32 v88, v88\n"
      "v_exp_f32 v89, v89\n"
      "v_mfma_f32_32x32x16_bf16 v[40:55], v[152:155], v[160:163], v[40:55]\n"
      "s_cmp_lt_u32 %[st], %[conf]\n"
      "s_cbranch_scc1 GMCC%=\n"
      "s_waitcnt vmcnt(3)\n"
      "GMRC%=:\n"
      "v_exp_f32 v90, v90\n"
      "v_exp_f32 v91, v91\n"
      "v_exp_f32 v92, v92\n"
      "v_exp_f32 v93, v93\n"
      "v_exp_f32 v94, v94\n"
      "v_mfma_f32_32x32x16_bf16 v[72:87], v[120:123], v[104:107], 0\n"
      "v_exp_f32 v95, v95\n"
      "v_cvt_pk_bf16_f32 v156, v88, v89\n"
      "v_cvt_pk_bf16_f32 v157, v90, v91\n"
      "v_cvt_pk_bf16_f32 v158, v92, v93\n"
      "v_cvt_pk_bf16_f32 v159, v94, v95\n"
      "v_exp_f32 v96, v96\n"
      "v_exp_f32 v97, v97\n"
      "v_mfma_f32_32x32x16_bf16 v[56:71], v[148:151], v[156:159], v[56:71]\n"
      "v_exp_f32 v98, v98\n"
      "v_exp_f32 v99, v99\n"
      "v_exp_f32 v100, v100\n"
      "v_exp_f32 v101, v101\n"
      "v_exp_f32 v102, v102\n"
      "v_exp_f32 v103, v103\n"
      "v_cvt_pk_bf16_f32 v160, v96, v97\n"
      "v_cvt_pk_bf16_f32 v161, v98, v99\n"
      "v_cvt_pk_bf16_f32 v162, v100, v101\n"
      "v_cvt_pk_bf16_f32 v163, v102, v103\n"
      "v_exp_f32 v72, v72\n"
      "v_exp_f32 v73, v73\n"
      "v_mfma_f32_32x32x16_bf16 v[56:71], v[152:155], v[160:163], v[56:71]\n"
      "s_add_u32 %[so], %[so], 0x2000\n"
      "s_bitset0_b32 %[so], 17\n"
      "v_exp_f32 v74, v74\n"
      "v_exp_f32 v75, v75\n"
      "v_exp_f32 v76, v76\n"
      "v_exp_f32 v77, v77\n"
      "v_exp_f32 v78, v78\n"
      "v_mfma_f32_32x32x16_bf16 v[88:103], v[120:123], v[108:111], 0\n"
      "buffer_load_dwordx4 v[144:147], v164, %[srd], %[so] offen sc1\n"
      "s_mov_b64 exec, %[msk]\n"
      "buffer_load_dwordx4 v[148:151], v164, %[srd], %[so] offen offset:1024 sc1\n"
      "buffer_load_dwordx4 v[152:155], v164, %[srd], %[so] offen offset:1280 sc1\n"
      "s_mov_b64 exec, -1\n"
      "v_exp_f32 v79, v79\n"
      "v_cvt_pk_bf16_f32 v156, v72, v73\n"
      "v_cvt_pk_bf16_f32 v157, v74, v75\n"
      "v_cvt_pk_bf16_f32 v158, v76, v77\n"
      "v_cvt_pk_bf16_f32 v159, v78, v79\n"
      "v_exp_f32 v80, v80\n"
      "v_exp_f32 v81, v81\n"
      "v_mfma_f32_32x32x16_bf16 v[8:23], v[124:127], v[156:159], v[8:23]\n"
      "v_exp_f32 v82, v82\n"
      "v_exp_f32 v83, v83\n"
      "v_exp_f32 v84, v84\n"
      "v_exp_f32 v85, v85\n"
      "v_exp_f32 v86, v86\n"
      "v_exp_f32 v87, v87\n"
      "v_cvt_pk_bf16_f32 v160, v80, v81\n"
      "v_cvt_pk_bf16_f32 v161, v82, v83\n"
      "v_cvt_pk_bf16_f32 v162, v84, v85\n"
      "v_cvt_pk_bf16_f32 v163, v86, v87\n"
      "v_exp_f32 v88, v88\n"
      "v_exp_f32 v89, v89\n"
      "v_mfma_f32_32x32x16_bf16 v[8:23], v[128:131], v[160:163], v[8:23]\n"
      "v_exp_f32 v90, v90\n"
      "v_exp_f32 v91, v91\n"
      "v_exp_f32 v92, v92\n"
      "v_exp_f32 v93, v93\n"
      "v_exp_f32 v94, v94\n"
      "v_mfma_f32_32x32x16_bf16 v[72:87], v[120:123], v[112:115], 0\n"
      "v_exp_f32 v95, v95\n"
      "v_cvt_pk_bf16_f32 v156, v88, v89\n"
      "v_cvt_pk_bf16_f32 v157, v90, v91\n"
      "v_cvt_pk_bf16_f32 v158, v92, v93\n"
      "v_cvt_pk_bf16_f32 v159, v94, v95\n"
      "v_exp_f32 v96, v96\n"
      "v_exp_f32 v97, v97\n"
      "v_mfma_f32_32x32x16_bf16 v[24:39], v[124:127], v[156:159], v[24:39]\n"
      "v_exp_f32 v98, v98\n"
      "v_exp_f32 v99, v99\n"
      "v_exp_f32 v100, v100\n"
      "v_exp_f32 v101, v101\n"
      "v_exp_f32 v102, v102\n"
      "v_exp_f32 v103, v103\n"
      "v_cvt_pk_bf16_f32 v160, v96, v97\n"
      "v_cvt_pk_bf16_f32 v161, v98, v99\n"
      "v_cvt_pk_bf16_f32 v162, v100, v101\n"
      "v_cvt_pk_bf16_f32 v163, v102, v103\n"
      "v_exp_f32 v72, v72\n"
      "v_exp_f32 v73, v73\n"
      "v_mfma_f32_32x32x16_bf16 v[24:39], v[128:131], v[160:163], v[24:39]\n"
      "v_exp_f32 v74, v74\n"
      "v_exp_f32 v75, v75\n"
      "v_exp_f32 v76, v76\n"
      "v_exp_f32 v77, v77\n"
      "v_exp_f32 v78, v78\n"
      "v_mfma_f32_32x32x16_bf16 v[88:103], v[120:123], v[116:119], 0\n"
      "v_exp_f32 v79, v79\n"
      "v_cvt_pk_bf16_f32 v156, v72, v73\n"
      "v_cvt_pk_bf16_f32 v157, v74, v75\n"
      "v_cvt_pk_bf16_f32 v158, v76, v77\n"
      "v_cvt_pk_bf16_f32 v159, v78, v79\n"
      "v_exp_f32 v80, v80\n"
      "v_exp_f32 v81, v81\n"
      "v_mfma_f32_32x32x16_bf16 v[40:55], v[124:127], v[156:159], v[40:55]\n"
      "s_add_u32 %[st], %[st], 1\n"
      "v_exp_f32 v82, v82\n"
      "v_exp_f32 v83, v83\n"
      "v_exp_f32 v84, v84\n"
      "v_exp_f32 v85, v85\n"
      "v_exp_f32 v86, v86\n"
      "v_exp_f32 v87, v87\n"
      "v_cvt_pk_bf16_f32 v160, v80, v81\n"
      "v_cvt_pk_bf16_f32 v161, v82, v83\n"
      "v_cvt_pk_bf16_f32 v162, v84, v85\n"
      "v_cvt_pk_bf16_f32 v163, v86, v87\n"
      "v_exp_f32 v88, v88\n"
      "v_exp_f32 v89, v89\n"
      "v_mfma_f32_32x32x16_bf16 v[40:55], v[128:131], v[160:163], v[40:55]\n"
      "s_cmp_lt_u32 %[st], %[conf]\n"
      "s_cbranch_scc1 GMCA%=\n"
      "s_waitcnt vmcnt(3)\n"
      "GMRA%=:\n"
      "v_exp_f32 v90, v90\n"
      "v_exp_f32 v91, v91\n"
      "v_exp_f32 v92, v92\n"
      "v_exp_f32 v93, v93\n"
      "v_exp_f32 v94, v94\n"
      "v_mfma_f32_32x32x16_bf16 v[72:87], v[132:135], v[104:107], 0\n"
      "v_exp_f32 v95, v95\n"
      "v_cvt_pk_bf16_f32 v156, v88, v89\n"
      "v_cvt_pk_bf16_f32 v157, v90, v91\n"
      "v_cvt_pk_bf16_f32 v158, v92, v93\n"
      "v_cvt_pk_bf16_f32 v159, v94, v95\n"
      "v_exp_f32 v96, v96\n"
      "v_exp_f32 v97, v97\n"
      "v_mfma_f32_32x32x16_bf16 v[56:71], v[124:127], v[156:159], v[56:71]\n"
      "v_exp_f32 v98, v98\n"
      "v_exp_f32 v99, v99\n"
      "v_exp_f32 v100, v100\n"
      "v_exp_f32 v101, v101\n"
      "v_exp_f32 v102, v102\n"
      "v_exp_f32 v103, v103\n"
      "v_cvt_pk_bf16_f32 v160, v96, v97\n"
      "v_cvt_pk_bf16_f32 v161, v98, v99\n"
      "v_cvt_pk_bf16_f32 v162, v100, v101\n"
      "v_cvt_pk_bf16_f32 v163, v102, v103\n"
      "s_cmp_lt_u32 %[st], 16\n"
      "s_cbranch_scc1 GMLOOP%=\n"
      "s_branch GMEND%=\n"
      "GMCZ%=:\n"
      "s_waitcnt vmcnt(3)\n"
      "s_add_u32 %[s1], %[so], 0x1e000\n"
      "s_bitset0_b32 %[s1], 17\n"
      "s_movk_i32 %[sp], 0x4000\n"
      "GMSZ%=:\n"
      "v_max3_u32 v156, v132, v133, v134\n"
      "v_max3_u32 v157, v135, v136, v137\n"
      "v_max3_u32 v158, v138, v139, v140\n"
      "v_max3_u32 v159, v141, v142, v143\n"
      "v_max3_u32 v156, v156, v157, v158\n"
      "v_max_u32 v156, v156, v159\n"
      "v_cmp_eq_u32 vcc, -1, v156\n"
      "s_cbranch_vccz GMOZ%=\n"
      "s_sub_u32 %[sp], %[sp], 1\n"
      "s_cbranch_scc1 GMOZ%=\n"
      "buffer_load_dwordx4 v[132:135], v164, %[srd], %[s1] offen sc1\n"
      "s_mov_b64 exec, %[msk]\n"
      "buffer_load_dwordx4 v[136:139], v164, %[srd], %[s1] offen offset:1024 sc1\n"
      "buffer_load_dwordx4 v[140:143], v164, %[srd], %[s1] offen offset:1280 sc1\n"
      "s_mov_b64 exec, -1\n"
      "s_waitcnt vmcnt(0)\n"
      "v_max3_u32 v156, v144, v145, v146\n"
      "v_max3_u32 v157, v147, v148, v149\n"
      "v_max3_u32 v158, v150, v151, v152\n"
      "v_max3_u32 v159, v153, v154, v155\n"
      "v_max3_u32 v156, v156, v157, v158\n"
      "v_max_u32 v156, v156, v159\n"
      "v_cmp_eq_u32 vcc, -1, v156\n"
      "s_cbranch_vccz GMSZ%=\n"
      "buffer_load_dwordx4 v[144:147], v164, %[srd], %[so] offen sc1\n"
      "s_mov_b64 exec, %[msk]\n"
      "buffer_load_dwordx4 v[148:151], v164, %[srd], %[so] offen offset:1024 sc1\n"
      "buffer_load_dwordx4 v[152:155], v164, %[srd], %[so] offen offset:1280 sc1\n"
      "s_mov_b64 exec, -1\n"
      "s_branch GMSZ%=\n"
      "GMOZ%=:\n"
      "buffer_load_dwordx2 v[166:167], v165, %[srd], %[flb] offen sc1\n"
      "s_branch GMRZ%=\n"
      "GMCA%=:\n"
      "s_waitcnt vmcnt(3)\n"
      "s_add_u32 %[s1], %[so], 0x1e000\n"
      "s_bitset0_b32 %[s1], 17\n"
      "s_movk_i32 %[sp], 0x4000\n"
      "GMSA%=:\n"
      "v_max3_u32 v156, v132, v133, v134\n"
      "v_max3_u32 v157, v135, v136, v137\n"
      "v_max3_u32 v158, v138, v139, v140\n"
      "v_max3_u32 v159, v141, v142, v143\n"
      "v_max3_u32 v156, v156, v157, v158\n"
      "v_max_u32 v156, v156, v159\n"
      "v_cmp_eq_u32 vcc, -1, v156\n"
      "s_cbranch_vccz GMOA%=\n"
      "s_sub_u32 %[sp], %[sp], 1\n"
      "s_cbranch_scc1 GMOA%=\n"
      "buffer_load_dwordx4 v[132:135], v164, %[srd], %[s1] offen sc1\n"
      "s_mov_b64 exec, %[msk]\n"
      "buffer_load_dwordx4 v[136:139], v164, %[srd], %[s1] offen offset:1024 sc1\n"
      "buffer_load_dwordx4 v[140:143], v164, %[srd], %[s1] offen offset:1280 sc1\n"
      "s_mov_b64 exec, -1\n"
      "s_waitcnt vmcnt(0)\n"
      "v_max3_u32 v156, v144, v145, v146\n"
      "v_max3_u32 v157, v147, v148, v149\n"
      "v_max3_u32 v158, v150, v151, v152\n"
      "v_max3_u32 v159, v153, v154, v155\n"
      "v_max3_u32 v156, v156, v157, v158\n"
      "v_max_u32 v156, v156, v159\n"
      "v_cmp_eq_u32 vcc, -1, v156\n"
      "s_cbranch_vccz GMSA%=\n"
      "buffer_load_dwordx4 v[144:147], v164, %[srd], %[so] offen sc1\n"
      "s_mov_b64 exec, %[msk]\n"
      "buffer_load_dwordx4 v[148:151], v164, %[srd], %[so] offen offset:1024 sc1\n"
      "buffer_load_dwordx4 v[152:155], v164, %[srd], %[so] offen offset:1280 sc1\n"
      "s_mov_b64 exec, -1\n"
      "s_branch GMSA%=\n"
      "GMOA%=:\n"
      "v_cmp_ne_u32 vcc, %[tag], v167\n"
      "s_cbranch_vccz GMFA%=\n"
      "buffer_load_dwordx2 v[166:167], v165, %[srd], %[flb] offen sc1\n"
      "s_branch GMRA%=\n"
      "GMFA%=:\n"
      "s_add_u32 %[sp], %[st], 2\n"
      "s_min_u32 %[conf], %[conf], %[sp]\n"
      "s_branch GMRA%=\n"
      "GMCB%=:\n"
      "s_waitcnt vmcnt(3)\n"
      "s_add_u32 %[s1], %[so], 0x1e000\n"
      "s_bitset0_b32 %[s1], 17\n"
      "s_movk_i32 %[sp], 0x4000\n"
      "GMSB%=:\n"
      "v_max3_u32 v156, v144, v145, v146\n"
      "v_max3_u32 v157, v147, v148, v149\n"
      "v_max3_u32 v158, v150, v151, v152\n"
      "v_max3_u32 v159, v153, v154, v155\n"
      "v_max3_u32 v156, v156, v157, v158\n"
      "v_max_u32 v156, v156, v159\n"
      "v_cmp_eq_u32 vcc, -1, v156\n"
      "s_cbranch_vccz GMOB%=\n"
      "s_sub_u32 %[sp], %[sp], 1\n"
      "s_cbranch_scc1 GMOB%=\n"
      "buffer_load_dwordx4 v[144:147], v164, %[srd], %[s1] offen sc1\n"
      "s_mov_b64 exec, %[msk]\n"
      "buffer_load_dwordx4 v[148:151], v164, %[srd], %[s1] offen offset:1024 sc1\n"
      "buffer_load_dwordx4 v[152:155], v164, %[srd], %[s1] offen offset:1280 sc1\n"
      "s_mov_b64 exec, -1\n"
      "s_waitcnt vmcnt(0)\n"
      "v_max3_u32 v156, v120, v121, v122\n"
      "v_max3_u32 v157, v123, v124, v125\n"
      "v_max3_u32 v158, v126, v127, v128\n"
      "v_max3_u32 v159, v129, v130, v131\n"
      "v_max3_u32 v156, v156, v157, v158\n"
      "v_max_u32 v156, v156, v159\n"
      "v_cmp_eq_u32 vcc, -1, v156\n"
      "s_cbranch_vccz GMSB%=\n"
      "buffer_load_dwordx4 v[120:123], v164, %[srd], %[so] offen sc1\n"
      "s_mov_b64 exec, %[msk]\n"
      "buffer_load_dwordx4 v[124:127], v164, %[srd], %[so] offen offset:1024 sc1\n"
      "buffer_load_dwordx4 v[128:131], v164, %[srd], %[so] offen offset:1280 sc1\n"
      "s_mov_b64 exec, -1\n"
      "s_branch GMSB%=\n"
      "GMOB%=:\n"
      "v_cmp_ne_u32 vcc, %[tag], v167\n"
      "s_cbranch_vccz GMFB%=\n"
      "buffer_load_dwordx2 v[166:167], v165, %[srd], %[flb] offen sc1\n"
      "s_branch GMRB%=\n"
      "GMFB%=:\n"
      "s_add_u32 %[sp], %[st], 2\n"
      "s_min_u32 %[conf], %[conf], %[sp]\n"
      "s_branch GMRB%=\n"
      "GMCC%=:\n"
      "s_waitcnt vmcnt(3)\n"
      "s_add_u32 %[s1], %[so], 0x1e000\n"
      "s_bitset0_b32 %[s1], 17\n"
      "s_movk_i32 %[sp], 0x4000\n"
      "GMSC%=:\n"
      "v_max3_u32 v156, v120, v121, v122\n"
      "v_max3_u32 v157, v123, v124, v125\n"
      "v_max3_u32 v158, v126, v127, v128\n"
      "v_max3_u32 v159, v129, v130, v131\n"
      "v_max3_u32 v156, v156, v157, v158\n"
      "v_max_u32 v156, v156, v159\n"
      "v_cmp_eq_u32 vcc, -1, v156\n"
      "s_cbranch_vccz GMOC%=\n"
      "s_sub_u32 %[sp], %[sp], 1\n"
      "s_cbranch_scc1 GMOC%=\n"
      "buffer_load_dwordx4 v[120:123], v164, %[srd], %[s1] offen sc1\n"
      "s_mov_b64 exec, %[msk]\n"
      "buffer_load_dwordx4 v[124:127], v164, %[srd], %[s1] offen offset:1024 sc1\n"
      "buffer_load_dwordx4 v[128:131], v164, %[srd], %[s1] offen offset:1280 sc1\n"
      "s_mov_b64 exec, -1\n"
      "s_waitcnt vmcnt(0)\n"
      "v_max3_u32 v156, v132, v133, v134\n"
      "v_max3_u32 v157, v135, v136, v137\n"
      "v_max3_u32 v158, v138, v139, v140\n"
      "v_max3_u32 v159, v141, v142, v143\n"
      "v_max3_u32 v156, v156, v157, v158\n"
      "v_max_u32 v156, v156, v159\n"
      "v_cmp_eq_u32 vcc, -1, v156\n"
      "s_cbranch_vccz GMSC%=\n"
      "buffer_load_dwordx4 v[132:135], v164, %[srd], %[so] offen sc1\n"
      "s_mov_b64 exec, %[msk]\n"
      "buffer_load_dwordx4 v[136:139], v164, %[srd], %[so] offen offset:1024 sc1\n"
      "buffer_load_dwordx4 v[140:143], v164, %[srd], %[so] offen offset:1280 sc1\n"
      "s_mov_b64 exec, -1\n"
      "s_branch GMSC%=\n"
      "GMOC%=:\n"
      "v_cmp_ne_u32 vcc, %[tag], v167\n"
      "s_cbranch_vccz GMFC%=\n"
      "buffer_load_dwordx2 v[166:167], v165, %[srd], %[flb] offen sc1\n"
      "s_branch GMRC%=\n"
      "GMFC%=:\n"
      "s_add_u32 %[sp], %[st], 2\n"
      "s_min_u32 %[conf], %[conf], %[sp]\n"
      "s_branch GMRC%=\n"
      "GMEND%=:\n"
      "s_nop 1\n"
      "v_mfma_f32_32x32x16_bf16 v[56:71], v[128:131], v[160:163], v[56:71]\n"
      "s_waitcnt vmcnt(0)\n"
      "s_nop 15\n"
          : "={v[8:23]}"(O0), "={v[24:39]}"(O1), "={v[40:55]}"(O2), "={v[56:71]}"(O3), "+{v[120:123]}"(KA), "+{v[124:127]}"(VA0),
            "+{v[128:131]}"(VA1), "+{v[166:167]}"(fl), [so] "+s"(so), [s1] "=&s"(s1), [st] "=&s"(st), [conf] "=&s"(conf),
            [sp] "=&s"(sp)
          : "{v[104:107]}"(Q0), "{v[108:111]}"(Q1), "{v[112:115]}"(Q2), "{v[116:119]}"(Q3), "{v164}"(l16), "{v165}"((lane & 15) * 128),
            [srd] "s"(sws), [msk] "s"(0x0000ffff0000ffffull), [km] "s"(km), [tag] "s"(tag), [flo] "s"(flb + t * 128),
            [flb] "s"(flb)
          : "memory", "scc", "vcc",
            "v72", "v73", "v74", "v75", "v76", "v77", "v78", "v79", "v80", "v81", "v82", "v83", "v84", "v85", "v86", "v87", "v88", "v89", "v90", "v91", "v92", "v93", "v94", "v95", "v96", "v97", "v98", "v99", "v100", "v101", "v102", "v103", "v132", "v133", "v134", "v135", "v136", "v137", "v138", "v139", "v140", "v141", "v142", "v143", "v144", "v145", "v146", "v147", "v148", "v149", "v150", "v151", "v152", "v153", "v154", "v155", "v156", "v157", "v158", "v159", "v160", "v161", "v162", "v163");
    }
    {
    const int lane = fresh_lane(), r = lane & 31, hh = lane >> 5;
    const unsigned l16 = lane * 16;
    PRIO(3);
#pragma unroll 1
    for (int i = 0; i < SPIN_MAX && __any(fl[1] != tag); ++i)
      asm volatile("buffer_load_dwordx2 %0, %1, %2, %3 offen sc1\n\ts_waitcnt vmcnt(0)" : "=&v"(fl) : "v"((lane & 15) * 128), "s"(sws), "s"(flb) : "memory");
    float m0 = 0.f, m1 = 0.f, m2 = 0.f, m3 = 0.f;
    {
      const bool safe = __all(qm * asf(fl[0]) <= SAFE2);
      if (!safe) {
        if (lane == 0) *lp<unsigned>(M_SLOW + hd * 4) = 1u;
        m0 = m1 = m2 = m3 = -3.0e38f;
#pragma unroll
        for (int i = 0; i < 16; ++i) { O0[i] = 0.f; O1[i] = 0.f; O2[i] = 0.f; O3[i] = 0.f; }
        const f32x16 z = {0, 0, 0, 0, 0, 0, 0, 0, 0, 0, 0, 0, 0, 0, 0, 0};
#pragma unroll 1
        for (int ss = 0; ss < 16; ++ss) {
          kv_load(KA, VA0, VA1, sws, l16, kvh + ss * 8192);
          asm volatile("s_waitcnt vmcnt(0)" : "+v"(KA), "+v"(VA0), "+v"(VA1)::"memory");
#define SLOW(Q, O, m)                                                                 \
  {                                                                                   \
    f32x16 S = MFMA(KA, Q, z);                                                        \
    float tm = S[0];                                                                  \
    _Pragma("unroll") for (int i = 1; i < 16; ++i) tm = fmaxf(tm, S[i]);              \
    tm = fmaxf(tm, xhalf(tm, lane));                                                        \
    const float mn = fmaxf(m, tm), al = __builtin_amdgcn_exp2f(m - mn);               \
    m = mn;                                                                           \
    _Pragma("unroll") for (int i = 0; i < 16; ++i) { O[i] *= al; S[i] = __builtin_amdgcn_exp2f(S[i] - mn); } \
    O = MFMA(VA0, pack8<0>(S), O);                                                    \
    O = MFMA(VA1, pack8<8>(S), O);                                                    \
  }
          SLOW(Q0, O0, m0) SLOW(Q1, O1, m1) SLOW(Q2, O2, m2) SLOW(Q3, O3, m3)
        }
      }
    }
    lwait<2>(M_OARM, 4);
    {
      const unsigned rb = L_R + hd * 27648;
#define RWR(O, m, j)                                                                                              \
  {                                                                                                               \
    const unsigned a = rb + (((tb + j) & 3) * 3 + (j - 1)) * 2304;                                                \
    f32x4 lo = {O[0], O[1], O[2], O[3]}, hi = {O[4], O[5], O[6], O[7]};                                           \
    *lp<f32x4>(a + l16) = lo;                                                                                \
    *lp<f32x4>(a + 1024 + l16) = hi;                                                                         \
    *lp<float>(a + 2048 + r * 8 + hh * 4) = hh ? m : O[8];                                                   \
  }
      RWR(O1, m1, 1) RWR(O2, m2, 2) RWR(O3, m3, 3)
      larrive(M_RSYNC + hd * 4);
      lwait<0>(M_RSYNC + hd * 4, 4);
      f32x4 lo[3], hi[3];
      f32x2 lm[3];
#pragma unroll
      for (int j = 0; j < 3; ++j) {
        const unsigned a = rb + (tb * 3 + j) * 2304;
        lo[j] = *lp<f32x4>(a + l16);
        hi[j] = *lp<f32x4>(a + 1024 + l16);
        lm[j] = *lp<f32x2>(a + 2048 + r * 8);
      }
      float l = O0[8];
      if (lword(M_SLOW + hd * 4)) {
        const float mm = fmaxf(fmaxf(m0, lm[0][1]), fmaxf(lm[1][1], lm[2][1]));
        const float a0 = __builtin_amdgcn_exp2f(m0 - mm);
        l *= a0;
#pragma unroll
        for (int i = 0; i < 8; ++i) O0[i] *= a0;
#pragma unroll
        for (int j = 0; j < 3; ++j) {
          const float aj = __builtin_amdgcn_exp2f(lm[j][1] - mm);
          lo[j] *= aj; hi[j] *= aj; lm[j][0] *= aj;
        }
      }
#pragma unroll
      for (int j = 0; j < 3; ++j) {
        l += lm[j][0];
#pragma unroll
        for (int i = 0; i < 4; ++i) { O0[i] += lo[j][i]; O0[4 + i] += hi[j][i]; }
      }
      const float il = __builtin_amdgcn_rcpf(l);
#pragma unroll
      for (int i = 0; i < 8; ++i) O0[i] *= il;
      st16c(nosent4(pack8<0>(O0)), sws, l16, WS_O + ((4 * t + tb) * 32 + head) * 1024);
      larrive(M_ADONE + hd * 4);
    }
    }
  } else {
    const int lane = fresh_lane(), r = lane & 31, hh = lane >> 5;
    const unsigned l16 = lane * 16;
    const unsigned xbase = r * 128 + ((hh ^ ((r >> 1) & 7)) << 4);
    const int tq = (lane & 15) >> 2, tp = lane & 3, tblk = (lane >> 4) & 1;
    const int m = w;
    const int rt = xcd * 4 + (idx >> 3), ct = idx & 7;
    PRIO(2);
    const u32x4 s1 = make_srd(w1, 512 * 1792 * 2);
    const unsigned v1o = (lane >> 2) * 3584 + (lane & 3) * 16;
    const unsigned s1o = p < 14 ? (128 * p + 32 * m) * 2 : OOB;
    unsigned k1 = (kc0 & 7) * 4;
#define W1OFF(ks) (s1o + k1 * 57344)
#define W1NEXT() { k1 = (k1 + 1) & 31; asm volatile("" : "+s"(k1)); }
    u32x4 w1r[6];
    {
      const u32x4 sx = make_srd(x, 2048 * 512 * 2), sw = make_srd(wqkv, 512 * 1536 * 2);
      const unsigned vE = (lane >> 3) * 1024 + (((lane & 7) ^ (lane >> 4)) << 4), vO = vE ^ 64;
      const unsigned vW = (lane >> 2) * 3072 + (lane & 3) * 16;
      const unsigned xo = (t * 128 + 32 * m) * 1024, wof = 16 * m * 3072 + p * 64;
      u32x4 wr[8][3];
#define ISSUEX(i)                                                                                       \
  _Pragma("unroll") for (int j = 0; j < 4; ++j)                                                         \
      dma16(sx, (j & 1) ? vO : vE, xo + j * 8192 + ((kc0 + (i)) & 7) * 128, L_X + (i) * 16384 + m * 4096 + j * 1024);
#define ISSUEW(i) _Pragma("unroll") for (int s = 0; s < 3; ++s) ld16(wr[i][s], sw, vW, wof + ((kc0 + (i)) & 7) * 196608 + s * 1024);
#pragma unroll
      for (int i = 0; i < LOOK; ++i) { ISSUEW(i) ISSUEX(i) }
      const unsigned hsel = (lane >> 1) & 1;
      const unsigned da = L_WR + (16 * m + (lane >> 2)) * 64;
      const unsigned dq = da + hsel * 4096 + hsel * 32 + (lane & 1) * 16, dk = dq ^ 32, dv = da + 8192 + (lane & 3) * 16;
#pragma unroll
      for (int i = 0; i < 8; ++i) {
        if (i + LOOK < 8) { ISSUEW(i + LOOK) }
        asm volatile("s_waitcnt vmcnt(%3)" : "+v"(wr[i][0]), "+v"(wr[i][1]), "+v"(wr[i][2]) : "i"((i + LOOK < 8 ? 7 * (i + LOOK) + 3 : 56) - 7 * i - 3) : "memory");
        lwr16(dq + (i & 1) * 12288, wr[i][0]);
        lwr16(dk + (i & 1) * 12288, wr[i][1]);
        lwr16(dv + (i & 1) * 12288, wr[i][2]);
        if (i + LOOK < 8) { ISSUEX(i + LOOK) }
        asm volatile("s_waitcnt vmcnt(%0)" ::"i"((i + LOOK < 8 ? 7 * (i + LOOK) + 7 : 56) - 7 * i - 7) : "memory");
        bar();
      }
#pragma unroll
      for (int j = 0; j < 6; ++j) { ld16(w1r[j], s1, v1o, W1OFF(j)); W1NEXT() }
    }
    const unsigned tag = load_tag(ws);
    if (VSPLIT) bar();
    bar();
    lwait<0>(M_QRD, 8);
    asm volatile("s_waitcnt vmcnt(0)" : "+v"(w1r[0]), "+v"(w1r[1]), "+v"(w1r[2]), "+v"(w1r[3]), "+v"(w1r[4]), "+v"(w1r[5])::"memory");
    const u32x4 sent = {0xffffffffu, 0xffffffffu, 0xffffffffu, 0xffffffffu};
#define OARM() { st16c(sent, sws, l16, WS_O + ((4 * t + m) * 32 + 2 * p) * 1024); st16c(sent, sws, l16, WS_O + ((4 * t + m) * 32 + 2 * p + 1) * 1024); }
    if (p < 14) {
      const unsigned ring = L_W1 + m * 7168;
#define W1DMA(ks) { dma16(s1, v1o, W1OFF(ks), ring + ((ks) % 7) * 1024); W1NEXT() }
#pragma unroll
      for (int j = 0; j < 6; ++j) lwr16(ring + j * 1024 + l16, w1r[j]);
      f32x16 a0, a1, a2, a3;
      {
        const u32x2* bp = (const u32x2*)(b1 + 128 * p + 32 * m + 4 * hh);
#pragma unroll
        for (int q = 0; q < 4; ++q) {
          u32x2 v = bp[2 * q];
          a0[4 * q] = bflo(v[0]); a0[4 * q + 1] = bfhi(v[0]); a0[4 * q + 2] = bflo(v[1]); a0[4 * q + 3] = bfhi(v[1]);
        }
        a1 = a0; a2 = a0; a3 = a0;
      }
      unsigned xa[2][4], wa = ring + (8 * hh + tq) * 64 + tblk * 32 + tp * 8;
#pragma unroll
      for (int s = 0; s < 4; ++s) { xa[0][s] = L_X + (xbase ^ (s << 5)); xa[1][s] = xa[0][s] + 65536; asm volatile("" : "+v"(xa[0][s]), "+v"(xa[1][s])); }
      asm volatile("" : "+v"(wa));
      u32x4 fw[2], fx[2][4];
#define HREAD(ks)                                                                                   \
  {                                                                                                 \
    fw[(ks) & 1] = ltr(wa + ((ks) % 7) * 1024, 256);                                                \
    _Pragma("unroll") for (int j = 0; j < 4; ++j)                                                   \
        fx[(ks) & 1][j] = lrd16(xa[(ks) >> 4][(ks) & 3] + (((ks) >> 2) & 3) * 16384 + j * 4096);    \
  }
      HREAD(0)
      SB;
      PRIO(HPRIO);
#pragma unroll
      for (int ks = 0; ks < HSTEPS; ++ks) {
        if (ks + 1 < 32) {
          asm volatile("s_waitcnt vmcnt(%0)" ::"i"(30 - ks < 4 ? 30 - ks : 4) : "memory");
          HREAD(ks + 1)
          if (ks + 6 < 32) W1DMA(ks + 6);
        }
        SB;
        a0 = MFMA(fw[ks & 1], fx[ks & 1][0], a0); a1 = MFMA(fw[ks & 1], fx[ks & 1][1], a1);
        a2 = MFMA(fw[ks & 1], fx[ks & 1][2], a2); a3 = MFMA(fw[ks & 1], fx[ks & 1][3], a3);
        SB;
      }
      PRIO(2);
      larrive(M_MSYNC);
#define HOUT(a, tbb)                                                                                                          \
  {                                                                                                                           \
    _Pragma("unroll") for (int i = 0; i < 16; ++i) { float v = __builtin_amdgcn_fmed3f(a[i], 0.f, __builtin_inff()); a[i] = v * v; } \
    const unsigned o = WS_H + ((4 * t + tbb) * 112 + 8 * p + 2 * m) * 1024;                                                   \
    st16c(pack8<0>(a), sws, l16, o);                                                                                          \
    st16c(pack8<8>(a), sws, l16, o + 1024);                                                                                   \
  }
      HOUT(a0, 0) HOUT(a1, 1) HOUT(a2, 2) HOUT(a3, 3)
    } else if (p == 14) {
      f32x16 a;
#pragma unroll
      for (int i = 0; i < 16; ++i) a[i] = 0.f;
#pragma unroll 1
      for (int c = 0; c < 8; ++c) {
#pragma unroll
        for (int s = 0; s < 4; ++s) {
          u32x4 gf = *(const u32x4*)(gw + (4 * ((kc0 + c) & 7) + s) * 16 + hh * 8);
          u32x4 xf = lrd16(L_X + (xbase ^ (s << 5)) + c * 16384 + m * 4096);
          a = MFMA(gf, xf, a);
        }
      }
      larrive(M_MSYNC);
      const float g = __builtin_amdgcn_rcpf(1.f + __builtin_amdgcn_exp2f(-1.4426950408889634f * a[0]));
      if (lane < 32) st4c(asu(g), sws, lane * 4, WS_G + (128 * t + 32 * m) * 4);
    } else {
      larrive(M_MSYNC);
    }
    OARM()
    VMCNT(0);
    if (lane < 32) st4c(tag, sws, lane * 4, WS_HF + (t * 64 + 4 * p + m) * 128);
    larrive(M_OARM);
    lwait<2>(M_MSYNC, 4);

    const int rb = m >> 1, cb = m & 1;
    const unsigned dvo = (lane >> 3) * 1024 + (((lane & 7) ^ (((lane >> 4) & 1) << 2)) << 4);
    const unsigned trp0 = (4 * hh + tq) * 128 + ((cb ^ (tq >> 1)) << 6) + tblk * 32 + tp * 8;
    f32x16 c0, c1, c2, c3;
    {
      const u32x4 s2 = make_srd(w2, 1792 * 512 * 2);
      const unsigned ring = L_W2 + m * 16384;
      const unsigned so = 28 * m * 16384 + ct * 128;
      int kk = 7 * (rt & 3) + ((7 * ct) >> 1);
      kk = kk >= 28 ? kk - 28 : kk;
      int kw = kk;
#pragma unroll
      for (int j = 0; j < 6; ++j) {
        dma16(s2, dvo, so + kw * 16384, ring + j * 2048);
        dma16(s2, dvo, so + kw * 16384 + 8192, ring + j * 2048 + 1024);
        kw = kw == 27 ? 0 : kw + 1;
      }
      {
        const unsigned fo = (lane < 14 ? 14 * m + lane : 56 + 2 * (rt & 1) + rb) * 128;
#pragma unroll 1
        for (int i = 0; i < SPIN_MAX; ++i) {
          if (__all(ld4c_wait(sws, fo, WS_HF + (rt >> 1) * 8192) == tag)) break;
          __builtin_amdgcn_s_sleep(8);
        }
      }
      const unsigned h0 = WS_H + ((2 * rt + rb) * 112 + 28 * m) * 1024, h1 = WS_H + ((2 * rt + (rb ^ 1)) * 112 + 28 * m) * 1024;
      u32x4 ha[7], hb[7], f0[2], f1[2];
#pragma unroll
      for (int j = 0; j < 6; ++j) {
        ld16c(ha[j], sws, l16, h0 + kk * 1024); ld16c(hb[j], sws, l16, h1 + kk * 1024);
        kk = kk == 27 ? 0 : kk + 1;
      }
#pragma unroll
      for (int i = 0; i < 16; ++i) { c0[i] = 0.f; c1[i] = 0.f; c2[i] = 0.f; c3[i] = 0.f; }
      unsigned wa0 = ring + trp0, wa1 = ring + (trp0 ^ 64);
      asm volatile("" : "+v"(wa0), "+v"(wa1));
      f0[0] = ltr(wa0, 1024); f1[0] = ltr(wa1, 1024);
      SB;
      PRIO(BPRIO);
#pragma unroll
      for (int ks = 0; ks < BSTEPS; ++ks) {
        const int j = ks % 7, j1 = (ks + 1) % 7, jn = (ks + 6) % 7;
        const int issued = ks < 22 ? ks : 22;
        const int nh = ks < 6 ? (5 - ks) * 2 + 4 * issued : 4 * (issued - (ks - 6) - 1);
        const int nw = ks + 1 < 6 ? 63 : (ks + 1 < 28 ? 4 * (issued - (ks - 5) - 1) + 2 : 63);
        asm volatile("s_waitcnt vmcnt(%2)" : "+v"(ha[j]), "+v"(hb[j]) : "i"(nh < nw ? nh : nw) : "memory");
        if (ks + 1 < 28) { f0[(ks + 1) & 1] = ltr(wa0 + j1 * 2048, 1024); f1[(ks + 1) & 1] = ltr(wa1 + j1 * 2048, 1024); }
        if (ks + 6 < 28) {
          dma16(s2, dvo, so + kk * 16384, ring + jn * 2048);
          dma16(s2, dvo, so + kk * 16384 + 8192, ring + jn * 2048 + 1024);
          ld16c(ha[jn], sws, l16, h0 + kk * 1024);
          ld16c(hb[jn], sws, l16, h1 + kk * 1024);
          kk = kk == 27 ? 0 : kk + 1;
        }
        SB;
        c0 = MFMA(f0[ks & 1], ha[j], c0); c1 = MFMA(f1[ks & 1], ha[j], c1); c2 = MFMA(f0[ks & 1], hb[j], c2); c3 = MFMA(f1[ks & 1], hb[j], c3);
        SB;
      }
      PRIO(2);
      VMCNT(0);
    }
    LGKM0;
    {
      const u32x4 so_ = make_srd(wo, 512 * 512 * 2);
#pragma unroll
      for (int j = 0; j < 16; ++j) dma16(so_, dvo, (128 * m + 8 * j) * 1024 + ct * 128, L_W2 + m * 16384 + j * 1024);
    }
    const unsigned xme = (m == 3 ? 55296u : L_W1 + m * 8192) + l16;
#define XWR(c, o)                                                                                  \
  _Pragma("unroll") for (int q = 0; q < 4; ++q) {                                                  \
    f32x4 v = {c[4 * q], c[4 * q + 1], c[4 * q + 2], c[4 * q + 3]};                                \
    *lp<f32x4>(xme + o + q * 1024) = v;                                                            \
  }
#define XADD(c, a)                                                                                 \
  _Pragma("unroll") for (int q = 0; q < 4; ++q) {                                                  \
    f32x4 v = *lp<f32x4>(a + q * 1024);                                                            \
    c[4 * q] += v[0]; c[4 * q + 1] += v[1]; c[4 * q + 2] += v[2]; c[4 * q + 3] += v[3];            \
  }
    XWR(c2, 0) XWR(c3, 4096)
    const float g = asf(ld4c_wait(sws, r * 4, WS_G + (64 * rt + 32 * rb) * 4));
    larrive(M_MSYNC);
#pragma unroll 1
    for (int i = 0; i < SPIN_MAX; ++i) {
      if (__all(ld4c_wait(sws, lane * 128, WS_HF + (rt >> 1) * 8192) == tag)) break;
      __builtin_amdgcn_s_sleep(8);
    }
    lwait<1>(M_MSYNC, 8);
    {
      const unsigned xo2 = ((m ^ 2) == 3 ? 55296u : L_W1 + (m ^ 2) * 8192) + l16;
      XADD(c0, xo2) XADD(c1, xo2 + 4096)
      larrive(M_MSYNC);
      const u32x2* bp = (const u32x2*)(b2 + 64 * ct + 32 * cb + 4 * hh);
      const float og = 1.f - g;
#pragma unroll
      for (int q = 0; q < 4; ++q) {
        u32x2 v = bp[2 * q];
        const unsigned v0 = v[0], v1 = v[1];
        c0[4 * q] += bflo(v0); c0[4 * q + 1] += bfhi(v0); c0[4 * q + 2] += bflo(v1); c0[4 * q + 3] += bfhi(v1);
      }
#pragma unroll
      for (int i = 0; i < 16; ++i) { c0[i] *= og; c1[i] *= og; c2[i] = 0.f; c3[i] = 0.f; }
    }
#pragma unroll 1
    for (int par = 0; par < 2; ++par) {
      lwait<6>(M_ADONE + par * 4, 4);
      u32x4 of[8];
      const unsigned ob = WS_O + ((2 * rt + rb) * 32 + 16 * cb + par) * 1024;
#pragma unroll 1
      for (int i = 0; i < SPIN_MAX; ++i) {
#pragma unroll
        for (int j = 0; j < 8; ++j) ld16c(of[j], sws, l16, ob + j * 2048);
        asm volatile("s_waitcnt vmcnt(0)"
                     : "+v"(of[0]), "+v"(of[1]), "+v"(of[2]), "+v"(of[3]), "+v"(of[4]), "+v"(of[5]), "+v"(of[6]), "+v"(of[7])::"memory");
        unsigned mx = 0;
#pragma unroll
        for (int j = 0; j < 8; ++j) mx = umax4(of[j], mx);
        if (!__any(mx == 0xffffffffu)) break;
        if (lword(M_ADONE + 4) < 4) __builtin_amdgcn_s_sleep(40);
      }
      const unsigned wb = L_W2 + cb * 32768 + par * 2048;
#pragma unroll
      for (int j = 0; j < 8; ++j) {
        u32x4 f0 = ltr(wb + j * 4096 + trp0, 1024), f1 = ltr(wb + j * 4096 + (trp0 ^ 64), 1024);
        c3 = MFMA(f1, of[j], c3);
        c2 = MFMA(f0, of[j], c2);
      }
    }
    {
      lwait<0>(M_MSYNC, 12);
#pragma unroll
      for (int i = 0; i < 16; ++i) c1[i] += g * c3[i];
      XWR(c1, 0)
      larrive(M_MSYNC);
#pragma unroll
      for (int i = 0; i < 16; ++i) c0[i] += g * c2[i];
      lwait<0>(M_MSYNC, 16);
      const unsigned xo1 = ((m ^ 1) == 3 ? 55296u : L_W1 + (m ^ 1) * 8192) + l16;
      XADD(c0, xo1)
      unsigned o[8];
#pragma unroll
      for (int i = 0; i < 8; ++i) o[i] = pk2(c0[2 * i], c0[2 * i + 1]);
      const u32x4 sout = make_srd(out, 2048 * 512 * 2);
      const unsigned vo = r * 1024 + hh * 16;
      const unsigned so = (64 * rt + 32 * rb) * 1024 + (64 * ct + 32 * cb) * 2;
#pragma unroll
      for (int k = 0; k < 2; ++k) {
        auto rx = __builtin_amdgcn_permlane32_swap(o[4 * k], o[4 * k + 2], false, false);
        auto ry = __builtin_amdgcn_permlane32_swap(o[4 * k + 1], o[4 * k + 3], false, false);
        u32x4 v = {rx[0], ry[0], rx[1], ry[1]};
        st16(v, sout, vo + k * 32, so);
      }
    }
    if (blockIdx.x == 0 && m == 0 && lane == 0) st4c(tag, sws, 0, WS_E);
  }
}

extern "C" void kernel_launch(void* const* d_in, const int* in_sizes, int n_in, void* d_out, int out_size, void* d_ws,
                              size_t ws_size, hipStream_t stream) {
  gm_kernel<<<256, 768, 0, stream>>>((const u16*)d_in[0], (const u16*)d_in[1], (const u16*)d_in[2], (const u16*)d_in[4],
                                     (const u16*)d_in[5], (const u16*)d_in[6], (const u16*)d_in[7], (const u16*)d_in[8],
                                     (u16*)d_out, d_ws);
}
```

```cpp
#include <hip/hip_runtime.h>
#include <stdint.h>

typedef __attribute__((ext_vector_type(8))) __bf16 bf16x8;
typedef __attribute__((ext_vector_type(4))) __bf16 bf16x4;
typedef __attribute__((ext_vector_type(2))) __bf16 bf16x2;
typedef __attribute__((ext_vector_type(2))) float f32x2;
typedef __attribute__((ext_vector_type(4))) float f32x4;
typedef __attribute__((ext_vector_type(16))) float f32x16;
typedef __attribute__((ext_vector_type(4))) unsigned u32x4;
typedef __attribute__((ext_vector_type(2))) unsigned u32x2;
typedef unsigned short u16;
#define LAS __attribute__((address_space(3)))
#define DI __device__ __forceinline__
#define SB __builtin_amdgcn_sched_barrier(0)
#define MFMA(a, b, c) __builtin_amdgcn_mfma_f32_32x32x16_bf16(__builtin_bit_cast(bf16x8, a), __builtin_bit_cast(bf16x8, b), (c), 0, 0, 0)

constexpr unsigned WS_E = 0, WS_G = 8192, WS_HF = 0x10000, WS_FL = 0x40000;
constexpr unsigned WS_KV = 1u << 20, WS_O = 9u << 20, WS_H = 11u << 20, WS_BYTES = 18u << 20;
constexpr unsigned OOB = 0x7f000000u;
constexpr unsigned L_X = 0, L_WR = 131072, L_QX = 131072 + 16384, L_W1 = 131072, L_MISC = 159744;
constexpr unsigned L_RG = 57344, RGN = 24576;
constexpr unsigned M_HD = L_MISC, M_MSYNC = L_MISC + 36, M_ADONE = L_MISC + 40, M_RSYNC = L_MISC + 48,
                   M_PSYNC = L_MISC + 56;
constexpr int PD = 12;
constexpr int SPIN_MAX = 1 << 16;
constexpr float SAFE2 = 80.f * 80.f;

DI u32x4 make_srd(const void* p, unsigned bytes) {
  uint64_t a = (uint64_t)p;
  u32x4 r = {(unsigned)a, (unsigned)(a >> 32) & 0xffffu, bytes, 0x00020000u};
  return r;
}
DI void dma16(u32x4 srd, unsigned voff, unsigned soff, unsigned ldsaddr) {
  asm volatile("s_mov_b32 m0, %3\n\ts_nop 0\n\tbuffer_load_dwordx4 %0, %1, %2 offen lds" ::"v"(voff), "s"(srd), "s"(soff), "s"(ldsaddr) : "memory");
}
DI void ld16(u32x4& d, u32x4 srd, unsigned voff, unsigned soff) {
  asm volatile("buffer_load_dwordx4 %0, %1, %2, %3 offen" : "=v"(d) : "v"(voff), "s"(srd), "s"(soff) : "memory");
}
DI void ld16c(u32x4& d, u32x4 srd, unsigned voff, unsigned soff) {
  asm volatile("buffer_load_dwordx4 %0, %1, %2, %3 offen sc1" : "=v"(d) : "v"(voff), "s"(srd), "s"(soff) : "memory");
}
DI void ld4c(unsigned& d, u32x4 srd, unsigned voff, unsigned soff) {
  asm volatile("buffer_load_dword %0, %1, %2, %3 offen sc1" : "=v"(d) : "v"(voff), "s"(srd), "s"(soff) : "memory");
}
DI unsigned ld4c_wait(u32x4 srd, unsigned voff, unsigned soff) {
  unsigned d;
  asm volatile("buffer_load_dword %0, %1, %2, %3 offen sc1\n\ts_waitcnt vmcnt(0)" : "=&v"(d) : "v"(voff), "s"(srd), "s"(soff) : "memory");
  return d;
}
DI void st16c(u32x4 v, u32x4 srd, unsigned voff, unsigned soff) {
  asm volatile("buffer_store_dwordx4 %0, %1, %2, %3 offen sc1\n\ts_nop 1" ::"v"(v), "v"(voff), "s"(srd), "s"(soff) : "memory");
}
DI void st16(u32x4 v, u32x4 srd, unsigned voff, unsigned soff) {
  asm volatile("buffer_store_dwordx4 %0, %1, %2, %3 offen\n\ts_nop 1" ::"v"(v), "v"(voff), "s"(srd), "s"(soff) : "memory");
}
DI void st4c(unsigned v, u32x4 srd, unsigned voff, unsigned soff) {
  asm volatile("buffer_store_dword %0, %1, %2, %3 offen sc1" ::"v"(v), "v"(voff), "s"(srd), "s"(soff) : "memory");
}
#define VMCNT(n) asm volatile("s_waitcnt vmcnt(" #n ")" ::: "memory")
#define LGKM0 asm volatile("s_waitcnt lgkmcnt(0)" ::: "memory")
DI void bar() { asm volatile("s_waitcnt lgkmcnt(0)\n\ts_barrier" ::: "memory"); }

DI float asf(unsigned v) { return __builtin_bit_cast(float, v); }
DI unsigned asu(float v) { return __builtin_bit_cast(unsigned, v); }
DI bf16x2 asb(unsigned v) { return __builtin_bit_cast(bf16x2, v); }
DI unsigned pk2(float a, float b) { f32x2 v = {a, b}; return __builtin_bit_cast(unsigned, __builtin_convertvector(v, bf16x2)); }
DI f32x16 zero16() { f32x16 z = {0, 0, 0, 0, 0, 0, 0, 0, 0, 0, 0, 0, 0, 0, 0, 0}; return z; }
template <int O> DI u32x4 pack8(const f32x16& a) {
  u32x4 r = {pk2(a[O], a[O + 1]), pk2(a[O + 2], a[O + 3]), pk2(a[O + 4], a[O + 5]), pk2(a[O + 6], a[O + 7])};
  return r;
}
DI unsigned umax3(unsigned a, unsigned b, unsigned c) { unsigned m = a > b ? a : b; return m > c ? m : c; }
DI unsigned umax4(u32x4 v, unsigned m) { return umax3(umax3(m, v[0], v[1]), v[2], v[3]); }
DI float bflo(unsigned v) { return asf(v << 16); }
DI float bfhi(unsigned v) { return asf(v & 0xffff0000u); }
DI int fresh_lane() { int l; asm volatile("v_mbcnt_lo_u32_b32 %0, -1, 0\n\tv_mbcnt_hi_u32_b32 %0, -1, %0" : "=v"(l)); return l; }
DI float xhalf(float v, int lane) {
  unsigned u = asu(v);
  auto r = __builtin_amdgcn_permlane32_swap(u, u, false, false);
  return asf((lane & 32) ? r[0] : r[1]);
}

template <int C, int RM> DI unsigned dppmax(unsigned v) {
  unsigned o = __builtin_amdgcn_update_dpp(0u, v, C, RM, 0xf, false);
  return v > o ? v : o;
}
DI unsigned half_umax(unsigned v) {
  v = dppmax<0x111, 0xf>(v); v = dppmax<0x112, 0xf>(v); v = dppmax<0x114, 0xf>(v); v = dppmax<0x118, 0xf>(v);
  return dppmax<0x142, 0xa>(v);
}
DI float sumsq(u32x4 v, float c) {
#pragma unroll
  for (int i = 0; i < 4; ++i) c = __builtin_amdgcn_fdot2_f32_bf16(asb(v[i]), asb(v[i]), c, false);
  return c;
}
#define PRIO(n) __builtin_amdgcn_s_setprio(n)

DI unsigned load_tag(const void* ws) {
  unsigned e;
  asm volatile("s_load_dword %0, %1, 0x0\n\ts_waitcnt lgkmcnt(0)" : "=s"(e) : "s"(ws) : "memory");
  return e + 1u;
}
__shared__ __attribute__((aligned(16))) char lds[163840];
template <class T> DI T* lp(unsigned a) { return (T*)(lds + a); }
DI u32x4 lrd16(unsigned a) { return *lp<u32x4>(a); }
DI void lwr16(unsigned a, u32x4 v) { *lp<u32x4>(a) = v; }
DI u32x4 ltr(unsigned a, unsigned d) {
  bf16x4 lo = __builtin_amdgcn_ds_read_tr16_b64_v4bf16((LAS bf16x4*)(lds + a));
  bf16x4 hi = __builtin_amdgcn_ds_read_tr16_b64_v4bf16((LAS bf16x4*)(lds + a + d));
  return __builtin_bit_cast(u32x4, __builtin_shufflevector(lo, hi, 0, 1, 2, 3, 4, 5, 6, 7));
}
DI unsigned lword(unsigned a) { return __builtin_amdgcn_readfirstlane(__hip_atomic_load(lp<unsigned>(a), __ATOMIC_RELAXED, __HIP_MEMORY_SCOPE_WORKGROUP)); }
DI void larrive(unsigned a, unsigned n = 1u) {
  asm volatile("s_mov_b64 exec, 1\n\tds_add_u32 %0, %1\n\ts_mov_b64 exec, -1" ::"v"(a), "v"(n) : "memory");
}
template <int SLP> DI void lwait(unsigned a, unsigned target) {
#pragma unroll 1
  for (int i = 0; i < (SPIN_MAX << 4); ++i) {
    if (lword(a) >= target) break;
    __builtin_amdgcn_s_sleep(SLP);
  }
  asm volatile("" ::: "memory");
}


DI void kv_load(u32x4& K, u32x4& V0, u32x4& V1, u32x4 srd, unsigned voff, unsigned soff) {
  asm volatile(
      "buffer_load_dwordx4 %0, %3, %4, %5 offen sc1\n\t"
      "s_mov_b64 exec, %6\n\t"
      "buffer_load_dwordx4 %1, %3, %4, %5 offen offset:1024 sc1\n\t"
      "buffer_load_dwordx4 %2, %3, %4, %5 offen offset:1280 sc1\n\t"
      "s_mov_b64 exec, -1"
      : "+v"(K), "+v"(V0), "+v"(V1)
      : "v"(voff), "s"(srd), "s"(soff), "s"(0x0000ffff0000ffffull)
      : "memory");
}
__global__ void __launch_bounds__(768, 3)
gm_kernel(const u16* x, const u16* wqkv, void* ws, const u16* w1, const u16* b1, const u16* w2, const u16* wo,
          const u16* gw, const u16* b2, u16* out) {
  const int w = __builtin_amdgcn_readfirstlane(threadIdx.x >> 6);
  const int xcd = blockIdx.x & 7, idx = blockIdx.x >> 3;
  if (w == 4) {
    unsigned dummy;
    asm volatile("s_getpc_b64 s[92:93]\n.Lgmpc:\n\ts_and_b32 s93, s93, 0xffff\n\ts_mov_b32 s94, .Lgmend-.Lgmpc\n\ts_mov_b32 s95, 0x20000\n\t"
                 "s_mov_b64 exec, 0x3ff\n\tbuffer_load_dword %0, %1, s[92:95], 0 offen\n\ts_mov_b64 exec, -1\n\ts_waitcnt vmcnt(0)"
                 : "=&v"(dummy) : "v"(((threadIdx.x & 63) * 32 + idx) * 128) : "s92", "s93", "s94", "s95", "scc", "memory");
  }
  const int t = (xcd >> 2) * 8 + (idx & 7), p = (xcd & 3) * 4 + (idx >> 3);
  const int kc0 = t + 2 * p;
  const u32x4 sws = make_srd(ws, WS_BYTES);
  asm volatile("s_nop 4" ::"s"(x), "s"(wqkv), "s"(w1), "s"(b1), "s"(w2), "s"(wo), "s"(gw), "s"(b2), "s"(out));

  if (w >= 4) {
    const int lane = fresh_lane(), r = lane & 31, hh = lane >> 5;
    const unsigned l16 = lane * 16;
    const unsigned xbase = r * 128 + ((hh ^ ((r >> 1) & 7)) << 4);
    const int tq = (lane & 15) >> 2, tp = lane & 3, tblk = (lane >> 4) & 1;
    const int a = w - 4, tb = a & 3, hd = a >> 2, head = 2 * p + hd;
    const unsigned kvh = WS_KV + head * 0x40000 + tb * 2048;
    PRIO(1);
    const unsigned tag = load_tag(ws);
    unsigned a42 = L_QX + hd * 4096 + tb * 1024 + l16, a43 = L_QX + hd * 4096 + ((tb + 1) & 3) * 1024 + l16,
             a44 = L_QX + hd * 4096 + ((tb + 2) & 3) * 1024 + l16, a45 = L_QX + hd * 4096 + ((tb + 3) & 3) * 1024 + l16, vof = l16,
             flo = r * 128 + hd * 8;
    const unsigned pad = (r & 27) == 16 ? 0x3f803f80u : 0u;
    u32x2 fl = {pad, pad};
    u32x4 misc = {0u, 0u, 2u, M_HD + hd * 16};
    asm volatile("" : "+{v42}"(a42), "+{v43}"(a43), "+{v44}"(a44), "+{v45}"(a45), "+{v164}"(vof), "+{v165}"(flo), "+{v[166:167]}"(fl),
                      "+{v[4:7]}"(misc));
    u32x4 KA, VA0, VA1, Q0, Q1, Q2, Q3;
    const unsigned flb = WS_FL + (p * 4 + tb) * 4096;
    {
      const int pc = (p & ~1) | hd;
      const unsigned kv0 = WS_KV + 2 * pc * 0x40000 + tb * 2048 + t * 8192;
      unsigned wa = L_WR + (8 * hh + tq) * 128 + ((hd ^ (tq >> 1)) << 6) + tblk * 32 + tp * 8, xa[2][4];
      unsigned wq = L_WR + (8 * hh + tq) * 64 + tblk * 32 + tp * 8;
#pragma unroll
      for (int s = 0; s < 4; ++s) { xa[0][s] = (L_X + tb * 4096 + xbase) ^ (s << 5); xa[1][s] = xa[0][s] + 65536; asm volatile("" : "+v"(xa[0][s]), "+v"(xa[1][s])); }
      asm volatile("" : "+v"(wa), "+v"(wq));
      f32x16 acc = zero16();
      u32x4 xf[8], wf[8], fv = {0u, tag, 0u, tag};
#define XRD(c, s) lrd16(xa[(c) >> 2][s] + ((c) & 3) * 16384)
#define PLOOP(A, B)                                                                                                     \
  _Pragma("unroll") for (int c = 0; c < 8; ++c) {                                                                       \
    bar();                                                                                                              \
    _Pragma("unroll") for (int s = 0; s < 4; ++s) { xf[s] = XRD(c, s); wf[s] = ltr(wa + (c % 3) * 8192 + s * 2048, 512); } \
    _Pragma("unroll") for (int s = 0; s < 4; ++s) acc = MFMA(A[s], B[s], acc);                                           \
    SB;                                                                                                                 \
  }
      if (!(p & 1)) {
        PLOOP(wf, xf)
        const u32x4 K0 = pack8<0>(acc);
        st16c(K0, sws, l16, kv0);
        const u32x4 K1 = pack8<8>(acc);
        st16c(K1, sws, l16, kv0 + 0x40000);
        SB;
        auto sw = __builtin_amdgcn_permlane32_swap(asu(sumsq(K0, 0.f)), asu(sumsq(K1, 0.f)), false, false);
        const unsigned nm = half_umax(asu(asf(sw[0]) + asf(sw[1])));
        fv[0] = __builtin_amdgcn_readlane(nm, 31); fv[2] = __builtin_amdgcn_readlane(nm, 63);
      } else {
        PLOOP(xf, wf)
        const u32x4 V0 = pack8<0>(acc), V1 = pack8<8>(acc);
        const unsigned vo = tblk * 0x40000 + 1024 + hh * 512 + (r & 15) * 16;
        st16c(V0, sws, vo, kv0);
        st16c(V1, sws, vo + 256, kv0);
        SB;
        fv[0] = fv[2] = __any(!(sumsq(V1, sumsq(V0, 0.f)) <= 1e12f)) ? 0x7f800000u : 0u;
      }
      bar();
      f32x16 aq = zero16();
#define QPAIR(j, b0, b1)                                                                                                \
  _Pragma("unroll") for (int s = 0; s < 8; ++s) { xf[s] = XRD(2 * (j) + (s >> 2), s & 3); wf[s] = ltr(wq + (b0) + s * 1024, 256); } \
  SB;                                                                                                                   \
  _Pragma("unroll") for (int s = 0; s < 8; ++s) {                                                                       \
    aq = MFMA(wf[s], xf[s], aq);                                                                                        \
    SB;                                                                                                                 \
    xf[s] = XRD(2 * (j) + 2 + (s >> 2), s & 3); wf[s] = ltr(wq + (b1) + s * 1024, 256);                                 \
    SB;                                                                                                                 \
  }                                                                                                                     \
  _Pragma("unroll") for (int s = 0; s < 8; ++s) aq = MFMA(wf[s], xf[s], aq);                                            \
  SB;
      if (hd == 0) { QPAIR(0, 24576, 16384) }
      VMCNT(0);
      if (lane < 8) st16c(fv, sws, l16, WS_FL + (pc * 4 + tb) * 4096 + ((p & 1) * 16 + t) * 128);
      bar();
      if (hd == 0) {
        QPAIR(2, 0, 8192)
        const float qs = 0.25f * 1.4426950408889634f;
#pragma unroll
        for (int i = 0; i < 16; ++i) aq[i] *= qs;
        lwr16(a42, pack8<0>(aq));
        lwr16(a42 + 4096, pack8<8>(aq));
      }
    }
    f32x16 O0, O1, O2, O3;
#define RADDR(d) ((hd * 4 + (d)) * 7168)
    unsigned st;
    {
      unsigned so = kvh + t * 8192, s1, rdy, sp;
      unsigned long long t0;
      asm volatile(
      "s_setprio 3\n"
      "v_mov_b64 v[124:125], v[166:167]\n"
      "v_mov_b64 v[126:127], v[166:167]\n"
      "v_mov_b64 v[128:129], v[166:167]\n"
      "v_mov_b64 v[130:131], v[166:167]\n"
      "v_mov_b64 v[136:137], v[166:167]\n"
      "v_mov_b64 v[138:139], v[166:167]\n"
      "v_mov_b64 v[140:141], v[166:167]\n"
      "v_mov_b64 v[142:143], v[166:167]\n"
      "v_mov_b64 v[148:149], v[166:167]\n"
      "v_mov_b64 v[150:151], v[166:167]\n"
      "v_mov_b64 v[152:153], v[166:167]\n"
      "v_mov_b64 v[154:155], v[166:167]\n"
      "s_cmp_eq_u32 %[hi], 0\n"
      "s_cbranch_scc1 GMJ0%=\n"
      "s_sleep 8\n"
      "GMJ0%=:\n"
      "buffer_load_dwordx2 v[166:167], v165, %[srd], %[flb] offen sc1\n"
      "s_cbranch_scc1 GMJ%=\n"
      "s_waitcnt vmcnt(0)\n"
      "s_bfe_u32 %[s1], %[so], 0x4000d\n"
      "s_mov_b32 %[sp], 0x10000\n"
      "GMEaL%=:\n"
      "v_cmp_eq_u32 vcc, %[tag], v167\n"
      "s_nop 0\n"
      "s_lshr_b32 %[rdy], vcc_lo, 16\n"
      "s_and_b32 %[rdy], %[rdy], vcc_lo\n"
      "s_bitcmp1_b32 %[rdy], %[s1]\n"
      "s_cbranch_scc1 GMEaO%=\n"
      "s_sub_u32 %[sp], %[sp], 1\n"
      "s_cbranch_scc1 GMEaO%=\n"
      "s_sleep 1\n"
      "s_not_b32 exec_lo, vcc_lo\n"
      "s_mov_b32 exec_hi, 0\n"
      "buffer_load_dwordx2 v[166:167], v165, %[srd], %[flb] offen sc1\n"
      "s_mov_b64 exec, -1\n"
      "s_waitcnt vmcnt(0)\n"
      "s_branch GMEaL%=\n"
      "GMEaO%=:\n"
      "buffer_load_dwordx4 v[120:123], v164, %[srd], %[so] offen sc1\n"
      "s_mov_b64 exec, %[msk]\n"
      "buffer_load_dwordx4 v[124:127], v164, %[srd], %[so] offen offset:1024 sc1\n"
      "buffer_load_dwordx4 v[128:131], v164, %[srd], %[so] offen offset:1280 sc1\n"
      "s_mov_b64 exec, -1\n"
      "s_add_u32 %[so], %[so], 0x2000\n"
      "s_bitset0_b32 %[so], 17\n"
      "s_bfe_u32 %[s1], %[so], 0x4000d\n"
      "s_mov_b32 %[sp], 0x10000\n"
      "GMFaL%=:\n"
      "v_cmp_eq_u32 vcc, %[tag], v167\n"
      "s_nop 0\n"
      "s_lshr_b32 %[rdy], vcc_lo, 16\n"
      "s_and_b32 %[rdy], %[rdy], vcc_lo\n"
      "s_bitcmp1_b32 %[rdy], %[s1]\n"
      "s_cbranch_scc1 GMFaO%=\n"
      "s_sub_u32 %[sp], %[sp], 1\n"
      "s_cbranch_scc1 GMFaO%=\n"
      "s_sleep 1\n"
      "s_not_b32 exec_lo, vcc_lo\n"
      "s_mov_b32 exec_hi, 0\n"
      "buffer_load_dwordx2 v[166:167], v165, %[srd], %[flb] offen sc1\n"
      "s_mov_b64 exec, -1\n"
      "s_waitcnt vmcnt(0)\n"
      "s_branch GMFaL%=\n"
      "GMFaO%=:\n"
      "buffer_load_dwordx4 v[132:135], v164, %[srd], %[so] offen sc1\n"
      "s_mov_b64 exec, %[msk]\n"
      "buffer_load_dwordx4 v[136:139], v164, %[srd], %[so] offen offset:1024 sc1\n"
      "buffer_load_dwordx4 v[140:143], v164, %[srd], %[so] offen offset:1280 sc1\n"
      "s_mov_b64 exec, -1\n"
      "GMJ%=:\n"
      "s_waitcnt lgkmcnt(0)\n"
      "s_barrier\n"
      "ds_read_b128 v[104:107], v42\n"
      "ds_read_b128 v[108:111], v43\n"
      "ds_read_b128 v[112:115], v44\n"
      "ds_read_b128 v[116:119], v45\n"
      "s_mov_b32 %[st], 1\n"
      "s_waitcnt lgkmcnt(0)\n"
      "s_barrier\n"
      "s_cmp_lg_u32 %[hi], 0\n"
      "s_cbranch_scc1 GMK%=\n"
      "s_waitcnt vmcnt(0)\n"
      "s_bfe_u32 %[s1], %[so], 0x4000d\n"
      "s_mov_b32 %[sp], 0x10000\n"
      "GMEbL%=:\n"
      "v_cmp_eq_u32 vcc, %[tag], v167\n"
      "s_nop 0\n"
      "s_lshr_b32 %[rdy], vcc_lo, 16\n"
      "s_and_b32 %[rdy], %[rdy], vcc_lo\n"
      "s_bitcmp1_b32 %[rdy], %[s1]\n"
      "s_cbranch_scc1 GMEbO%=\n"
      "s_sub_u32 %[sp], %[sp], 1\n"
      "s_cbranch_scc1 GMEbO%=\n"
      "s_sleep 1\n"
      "s_not_b32 exec_lo, vcc_lo\n"
      "s_mov_b32 exec_hi, 0\n"
      "buffer_load_dwordx2 v[166:167], v165, %[srd], %[flb] offen sc1\n"
      "s_mov_b64 exec, -1\n"
      "s_waitcnt vmcnt(0)\n"
      "s_branch GMEbL%=\n"
      "GMEbO%=:\n"
      "buffer_load_dwordx4 v[120:123], v164, %[srd], %[so] offen sc1\n"
      "s_mov_b64 exec, %[msk]\n"
      "buffer_load_dwordx4 v[124:127], v164, %[srd], %[so] offen offset:1024 sc1\n"
      "buffer_load_dwordx4 v[128:131], v164, %[srd], %[so] offen offset:1280 sc1\n"
      "s_mov_b64 exec, -1\n"
      "s_add_u32 %[so], %[so], 0x2000\n"
      "s_bitset0_b32 %[so], 17\n"
      "s_bfe_u32 %[s1], %[so], 0x4000d\n"
      "s_mov_b32 %[sp], 0x10000\n"
      "GMFbL%=:\n"
      "v_cmp_eq_u32 vcc, %[tag], v167\n"
      "s_nop 0\n"
      "s_lshr_b32 %[rdy], vcc_lo, 16\n"
      "s_and_b32 %[rdy], %[rdy], vcc_lo\n"
      "s_bitcmp1_b32 %[rdy], %[s1]\n"
      "s_cbranch_scc1 GMFbO%=\n"
      "s_sub_u32 %[sp], %[sp], 1\n"
      "s_cbranch_scc1 GMFbO%=\n"
      "s_sleep 1\n"
      "s_not_b32 exec_lo, vcc_lo\n"
      "s_mov_b32 exec_hi, 0\n"
      "buffer_load_dwordx2 v[166:167], v165, %[srd], %[flb] offen sc1\n"
      "s_mov_b64 exec, -1\n"
      "s_waitcnt vmcnt(0)\n"
      "s_branch GMFbL%=\n"
      "GMFbO%=:\n"
      "buffer_load_dwordx4 v[132:135], v164, %[srd], %[so] offen sc1\n"
      "s_mov_b64 exec, %[msk]\n"
      "buffer_load_dwordx4 v[136:139], v164, %[srd], %[so] offen offset:1024 sc1\n"
      "buffer_load_dwordx4 v[140:143], v164, %[srd], %[so] offen offset:1280 sc1\n"
      "s_mov_b64 exec, -1\n"
      "GMK%=:\n"
      "v_dot2_f32_bf16 v68, v104, v104, 0\n"
      "v_dot2_f32_bf16 v68, v105, v105, v68\n"
      "v_dot2_f32_bf16 v68, v106, v106, v68\n"
      "v_dot2_f32_bf16 v68, v107, v107, v68\n"
      "s_nop 2\n"
      "v_mov_b32 v69, v68\n"
      "s_nop 1\n"
      "v_permlane32_swap_b32 v68, v69\n"
      "s_nop 1\n"
      "v_add_f32 v68, v68, v69\n"
      "s_nop 1\n"
      "v_max_u32_dpp v68, v68, v68 row_shr:1 row_mask:0xf bank_mask:0xf bound_ctrl:1\n"
      "s_nop 1\n"
      "v_max_u32_dpp v68, v68, v68 row_shr:2 row_mask:0xf bank_mask:0xf bound_ctrl:1\n"
      "s_nop 1\n"
      "v_max_u32_dpp v68, v68, v68 row_shr:4 row_mask:0xf bank_mask:0xf bound_ctrl:1\n"
      "s_nop 1\n"
      "v_max_u32_dpp v68, v68, v68 row_shr:8 row_mask:0xf bank_mask:0xf bound_ctrl:1\n"
      "s_nop 1\n"
      "v_max_u32_dpp v68, v68, v68 row_bcast:15 row_mask:0xa bank_mask:0xf\n"
      "s_nop 1\n"
      "s_mov_b32 exec_lo, 0x80000000\n"
      "s_mov_b32 exec_hi, 0\n"
      "ds_max_u32 v7, v68 offset:8\n"
      "ds_add_u32 v7, v6 offset:4\n"
      "s_mov_b64 exec, -1\n"
      "s_waitcnt vmcnt(3)\n"
      "v_mfma_f32_32x32x16_bf16 v[72:87], v[120:123], v[104:107], 0\n"
      "s_setprio 1\n"
      "s_cmp_eq_u32 %[hi], 0\n"
      "s_cbranch_scc1 GMLO%=\n"
      "s_setprio 2\n"
      "GMLO%=:\n"
      "s_nop 9\n"
      "v_exp_f32 v72, v72\n"
      "v_exp_f32 v73, v73\n"
      "v_exp_f32 v74, v74\n"
      "v_exp_f32 v75, v75\n"
      "v_exp_f32 v76, v76\n"
      "v_exp_f32 v77, v77\n"
      "v_exp_f32 v78, v78\n"
      "v_mfma_f32_32x32x16_bf16 v[88:103], v[120:123], v[108:111], 0\n"
      "s_add_u32 %[so], %[so], 0x2000\n"
      "s_bitset0_b32 %[so], 17\n"
      "s_cmp_lg_u32 %[rdy], 0xffff\n"
      "s_cbranch_scc1 GMIZ%=\n"
      "GMIRZ%=:\n"
      "buffer_load_dwordx4 v[144:147], v164, %[srd], %[so] offen sc1\n"
      "s_mov_b64 exec, %[msk]\n"
      "buffer_load_dwordx4 v[148:151], v164, %[srd], %[so] offen offset:1024 sc1\n"
      "buffer_load_dwordx4 v[152:155], v164, %[srd], %[so] offen offset:1280 sc1\n"
      "s_mov_b64 exec, -1\n"
      "v_exp_f32 v79, v79\n"
      "v_cvt_pk_bf16_f32 v156, v72, v73\n"
      "v_cvt_pk_bf16_f32 v157, v74, v75\n"
      "v_cvt_pk_bf16_f32 v158, v76, v77\n"
      "v_cvt_pk_bf16_f32 v159, v78, v79\n"
      "v_exp_f32 v80, v80\n"
      "v_exp_f32 v81, v81\n"
      "v_mfma_f32_32x32x16_bf16 v[8:23], v[124:127], v[156:159], 0\n"
      "v_exp_f32 v82, v82\n"
      "v_exp_f32 v83, v83\n"
      "v_exp_f32 v84, v84\n"
      "v_exp_f32 v85, v85\n"
      "v_exp_f32 v86, v86\n"
      "v_exp_f32 v87, v87\n"
      "v_cvt_pk_bf16_f32 v160, v80, v81\n"
      "v_cvt_pk_bf16_f32 v161, v82, v83\n"
      "v_cvt_pk_bf16_f32 v162, v84, v85\n"
      "v_cvt_pk_bf16_f32 v163, v86, v87\n"
      "v_exp_f32 v88, v88\n"
      "v_exp_f32 v89, v89\n"
      "v_mfma_f32_32x32x16_bf16 v[8:23], v[128:131], v[160:163], v[8:23]\n"
      "v_exp_f32 v90, v90\n"
      "v_exp_f32 v91, v91\n"
      "v_exp_f32 v92, v92\n"
      "v_exp_f32 v93, v93\n"
      "v_exp_f32 v94, v94\n"
      "v_mfma_f32_32x32x16_bf16 v[72:87], v[120:123], v[112:115], 0\n"
      "v_exp_f32 v95, v95\n"
      "v_cvt_pk_bf16_f32 v156, v88, v89\n"
      "v_cvt_pk_bf16_f32 v157, v90, v91\n"
      "v_cvt_pk_bf16_f32 v158, v92, v93\n"
      "v_cvt_pk_bf16_f32 v159, v94, v95\n"
      "v_exp_f32 v96, v96\n"
      "v_exp_f32 v97, v97\n"
      "v_mfma_f32_32x32x16_bf16 v[24:39], v[124:127], v[156:159], 0\n"
      "v_exp_f32 v98, v98\n"
      "v_exp_f32 v99, v99\n"
      "v_exp_f32 v100, v100\n"
      "v_exp_f32 v101, v101\n"
      "v_exp_f32 v102, v102\n"
      "v_exp_f32 v103, v103\n"
      "v_cvt_pk_bf16_f32 v160, v96, v97\n"
      "v_cvt_pk_bf16_f32 v161, v98, v99\n"
      "v_cvt_pk_bf16_f32 v162, v100, v101\n"
      "v_cvt_pk_bf16_f32 v163, v102, v103\n"
      "v_exp_f32 v72, v72\n"
      "v_exp_f32 v73, v73\n"
      "v_mfma_f32_32x32x16_bf16 v[24:39], v[128:131], v[160:163], v[24:39]\n"
      "v_exp_f32 v74, v74\n"
      "v_exp_f32 v75, v75\n"
      "v_exp_f32 v76, v76\n"
      "v_exp_f32 v77, v77\n"
      "v_exp_f32 v78, v78\n"
      "v_mfma_f32_32x32x16_bf16 v[88:103], v[120:123], v[116:119], 0\n"
      "v_exp_f32 v79, v79\n"
      "v_cvt_pk_bf16_f32 v156, v72, v73\n"
      "v_cvt_pk_bf16_f32 v157, v74, v75\n"
      "v_cvt_pk_bf16_f32 v158, v76, v77\n"
      "v_cvt_pk_bf16_f32 v159, v78, v79\n"
      "v_exp_f32 v80, v80\n"
      "v_exp_f32 v81, v81\n"
      "v_mfma_f32_32x32x16_bf16 v[40:55], v[124:127], v[156:159], 0\n"
      "v_exp_f32 v82, v82\n"
      "v_exp_f32 v83, v83\n"
      "v_exp_f32 v84, v84\n"
      "v_exp_f32 v85, v85\n"
      "v_exp_f32 v86, v86\n"
      "v_exp_f32 v87, v87\n"
      "v_cvt_pk_bf16_f32 v160, v80, v81\n"
      "v_cvt_pk_bf16_f32 v161, v82, v83\n"
      "v_cvt_pk_bf16_f32 v162, v84, v85\n"
      "v_cvt_pk_bf16_f32 v163, v86, v87\n"
      "v_exp_f32 v88, v88\n"
      "v_exp_f32 v89, v89\n"
      "v_mfma_f32_32x32x16_bf16 v[40:55], v[128:131], v[160:163], v[40:55]\n"
      "s_waitcnt vmcnt(3)\n"
      "v_exp_f32 v90, v90\n"
      "v_exp_f32 v91, v91\n"
      "v_exp_f32 v92, v92\n"
      "v_exp_f32 v93, v93\n"
      "v_exp_f32 v94, v94\n"
      "v_mfma_f32_32x32x16_bf16 v[72:87], v[132:135], v[104:107], 0\n"
      "v_exp_f32 v95, v95\n"
      "v_cvt_pk_bf16_f32 v156, v88, v89\n"
      "v_cvt_pk_bf16_f32 v157, v90, v91\n"
      "v_cvt_pk_bf16_f32 v158, v92, v93\n"
      "v_cvt_pk_bf16_f32 v159, v94, v95\n"
      "v_exp_f32 v96, v96\n"
      "v_exp_f32 v97, v97\n"
      "v_mfma_f32_32x32x16_bf16 v[56:71], v[124:127], v[156:159], 0\n"
      "v_exp_f32 v98, v98\n"
      "v_exp_f32 v99, v99\n"
      "v_exp_f32 v100, v100\n"
      "v_exp_f32 v101, v101\n"
      "v_exp_f32 v102, v102\n"
      "v_exp_f32 v103, v103\n"
      "v_cvt_pk_bf16_f32 v160, v96, v97\n"
      "v_cvt_pk_bf16_f32 v161, v98, v99\n"
      "v_cvt_pk_bf16_f32 v162, v100, v101\n"
      "v_cvt_pk_bf16_f32 v163, v102, v103\n"
      "s_branch GMLB%=\n"
      "GMLOOP%=:\n"
      "v_exp_f32 v72, v72\n"
      "v_exp_f32 v73, v73\n"
      "v_mfma_f32_32x32x16_bf16 v[56:71], v[152:155], v[160:163], v[56:71]\n"
      "s_add_u32 %[so], %[so], 0x2000\n"
      "s_bitset0_b32 %[so], 17\n"
      "v_exp_f32 v74, v74\n"
      "v_exp_f32 v75, v75\n"
      "v_exp_f32 v76, v76\n"
      "v_exp_f32 v77, v77\n"
      "v_exp_f32 v78, v78\n"
      "v_mfma_f32_32x32x16_bf16 v[88:103], v[120:123], v[108:111], 0\n"
      "s_cmp_lg_u32 %[rdy], 0xffff\n"
      "s_cbranch_scc1 GMIA%=\n"
      "GMIRA%=:\n"
      "buffer_load_dwordx4 v[144:147], v164, %[srd], %[so] offen sc1\n"
      "s_mov_b64 exec, %[msk]\n"
      "buffer_load_dwordx4 v[148:151], v164, %[srd], %[so] offen offset:1024 sc1\n"
      "buffer_load_dwordx4 v[152:155], v164, %[srd], %[so] offen offset:1280 sc1\n"
      "s_mov_b64 exec, -1\n"
      "v_exp_f32 v79, v79\n"
      "v_cvt_pk_bf16_f32 v156, v72, v73\n"
      "v_cvt_pk_bf16_f32 v157, v74, v75\n"
      "v_cvt_pk_bf16_f32 v158, v76, v77\n"
      "v_cvt_pk_bf16_f32 v159, v78, v79\n"
      "v_exp_f32 v80, v80\n"
      "v_exp_f32 v81, v81\n"
      "v_mfma_f32_32x32x16_bf16 v[8:23], v[124:127], v[156:159], v[8:23]\n"
      "v_exp_f32 v82, v82\n"
      "v_exp_f32 v83, v83\n"
      "v_exp_f32 v84, v84\n"
      "v_exp_f32 v85, v85\n"
      "v_exp_f32 v86, v86\n"
      "v_exp_f32 v87, v87\n"
      "v_cvt_pk_bf16_f32 v160, v80, v81\n"
      "v_cvt_pk_bf16_f32 v161, v82, v83\n"
      "v_cvt_pk_bf16_f32 v162, v84, v85\n"
      "v_cvt_pk_bf16_f32 v163, v86, v87\n"
      "v_exp_f32 v88, v88\n"
      "v_exp_f32 v89, v89\n"
      "v_mfma_f32_32x32x16_bf16 v[8:23], v[128:131], v[160:163], v[8:23]\n"
      "v_exp_f32 v90, v90\n"
      "v_exp_f32 v91, v91\n"
      "v_exp_f32 v92, v92\n"
      "v_exp_f32 v93, v93\n"
      "v_exp_f32 v94, v94\n"
      "v_mfma_f32_32x32x16_bf16 v[72:87], v[120:123], v[112:115], 0\n"
      "v_exp_f32 v95, v95\n"
      "v_cvt_pk_bf16_f32 v156, v88, v89\n"
      "v_cvt_pk_bf16_f32 v157, v90, v91\n"
      "v_cvt_pk_bf16_f32 v158, v92, v93\n"
      "v_cvt_pk_bf16_f32 v159, v94, v95\n"
      "v_exp_f32 v96, v96\n"
      "v_exp_f32 v97, v97\n"
      "v_mfma_f32_32x32x16_bf16 v[24:39], v[124:127], v[156:159], v[24:39]\n"
      "v_exp_f32 v98, v98\n"
      "v_exp_f32 v99, v99\n"
      "v_exp_f32 v100, v100\n"
      "v_exp_f32 v101, v101\n"
      "v_exp_f32 v102, v102\n"
      "v_exp_f32 v103, v103\n"
      "v_cvt_pk_bf16_f32 v160, v96, v97\n"
      "v_cvt_pk_bf16_f32 v161, v98, v99\n"
      "v_cvt_pk_bf16_f32 v162, v100, v101\n"
      "v_cvt_pk_bf16_f32 v163, v102, v103\n"
      "v_exp_f32 v72, v72\n"
      "v_exp_f32 v73, v73\n"
      "v_mfma_f32_32x32x16_bf16 v[24:39], v[128:131], v[160:163], v[24:39]\n"
      "v_exp_f32 v74, v74\n"
      "v_exp_f32 v75, v75\n"
      "v_exp_f32 v76, v76\n"
      "v_exp_f32 v77, v77\n"
      "v_exp_f32 v78, v78\n"
      "v_mfma_f32_32x32x16_bf16 v[88:103], v[120:123], v[116:119], 0\n"
      "v_exp_f32 v79, v79\n"
      "v_cvt_pk_bf16_f32 v156, v72, v73\n"
      "v_cvt_pk_bf16_f32 v157, v74, v75\n"
      "v_cvt_pk_bf16_f32 v158, v76, v77\n"
      "v_cvt_pk_bf16_f32 v159, v78, v79\n"
      "v_exp_f32 v80, v80\n"
      "v_exp_f32 v81, v81\n"
      "v_mfma_f32_32x32x16_bf16 v[40:55], v[124:127], v[156:159], v[40:55]\n"
      "s_add_u32 %[st], %[st], 1\n"
      "v_exp_f32 v82, v82\n"
      "v_exp_f32 v83, v83\n"
      "v_exp_f32 v84, v84\n"
      "v_exp_f32 v85, v85\n"
      "v_exp_f32 v86, v86\n"
      "v_exp_f32 v87, v87\n"
      "v_cvt_pk_bf16_f32 v160, v80, v81\n"
      "v_cvt_pk_bf16_f32 v161, v82, v83\n"
      "v_cvt_pk_bf16_f32 v162, v84, v85\n"
      "v_cvt_pk_bf16_f32 v163, v86, v87\n"
      "v_exp_f32 v88, v88\n"
      "v_exp_f32 v89, v89\n"
      "v_mfma_f32_32x32x16_bf16 v[40:55], v[128:131], v[160:163], v[40:55]\n"
      "s_waitcnt vmcnt(3)\n"
      "v_exp_f32 v90, v90\n"
      "v_exp_f32 v91, v91\n"
      "v_exp_f32 v92, v92\n"
      "v_exp_f32 v93, v93\n"
      "v_exp_f32 v94, v94\n"
      "v_mfma_f32_32x32x16_bf16 v[72:87], v[132:135], v[104:107], 0\n"
      "v_exp_f32 v95, v95\n"
      "v_cvt_pk_bf16_f32 v156, v88, v89\n"
      "v_cvt_pk_bf16_f32 v157, v90, v91\n"
      "v_cvt_pk_bf16_f32 v158, v92, v93\n"
      "v_cvt_pk_bf16_f32 v159, v94, v95\n"
      "v_exp_f32 v96, v96\n"
      "v_exp_f32 v97, v97\n"
      "v_mfma_f32_32x32x16_bf16 v[56:71], v[124:127], v[156:159], v[56:71]\n"
      "v_exp_f32 v98, v98\n"
      "v_exp_f32 v99, v99\n"
      "v_exp_f32 v100, v100\n"
      "v_exp_f32 v101, v101\n"
      "v_exp_f32 v102, v102\n"
      "v_exp_f32 v103, v103\n"
      "v_cvt_pk_bf16_f32 v160, v96, v97\n"
      "v_cvt_pk_bf16_f32 v161, v98, v99\n"
      "v_cvt_pk_bf16_f32 v162, v100, v101\n"
      "v_cvt_pk_bf16_f32 v163, v102, v103\n"
      "GMLB%=:\n"
      "v_exp_f32 v72, v72\n"
      "v_exp_f32 v73, v73\n"
      "v_mfma_f32_32x32x16_bf16 v[56:71], v[128:131], v[160:163], v[56:71]\n"
      "s_add_u32 %[so], %[so], 0x2000\n"
      "s_bitset0_b32 %[so], 17\n"
      "v_exp_f32 v74, v74\n"
      "v_exp_f32 v75, v75\n"
      "v_exp_f32 v76, v76\n"
      "v_exp_f32 v77, v77\n"
      "v_exp_f32 v78, v78\n"
      "v_mfma_f32_32x32x16_bf16 v[88:103], v[132:135], v[108:111], 0\n"
      "s_cmp_lg_u32 %[rdy], 0xffff\n"
      "s_cbranch_scc1 GMIB%=\n"
      "GMIRB%=:\n"
      "buffer_load_dwordx4 v[120:123], v164, %[srd], %[so] offen sc1\n"
      "s_mov_b64 exec, %[msk]\n"
      "buffer_load_dwordx4 v[124:127], v164, %[srd], %[so] offen offset:1024 sc1\n"
      "buffer_load_dwordx4 v[128:131], v164, %[srd], %[so] offen offset:1280 sc1\n"
      "s_mov_b64 exec, -1\n"
      "v_exp_f32 v79, v79\n"
      "v_cvt_pk_bf16_f32 v156, v72, v73\n"
      "v_cvt_pk_bf16_f32 v157, v74, v75\n"
      "v_cvt_pk_bf16_f32 v158, v76, v77\n"
      "v_cvt_pk_bf16_f32 v159, v78, v79\n"
      "v_exp_f32 v80, v80\n"
      "v_exp_f32 v81, v81\n"
      "v_mfma_f32_32x32x16_bf16 v[8:23], v[136:139], v[156:159], v[8:23]\n"
      "v_exp_f32 v82, v82\n"
      "v_exp_f32 v83, v83\n"
      "v_exp_f32 v84, v84\n"
      "v_exp_f32 v85, v85\n"
      "v_exp_f32 v86, v86\n"
      "v_exp_f32 v87, v87\n"
      "v_cvt_pk_bf16_f32 v160, v80, v81\n"
      "v_cvt_pk_bf16_f32 v161, v82, v83\n"
      "v_cvt_pk_bf16_f32 v162, v84, v85\n"
      "v_cvt_pk_bf16_f32 v163, v86, v87\n"
      "v_exp_f32 v88, v88\n"
      "v_exp_f32 v89, v89\n"
      "v_mfma_f32_32x32x16_bf16 v[8:23], v[140:143], v[160:163], v[8:23]\n"
      "v_exp_f32 v90, v90\n"
      "v_exp_f32 v91, v91\n"
      "v_exp_f32 v92, v92\n"
      "v_exp_f32 v93, v93\n"
      "v_exp_f32 v94, v94\n"
      "v_mfma_f32_32x32x16_bf16 v[72:87], v[132:135], v[112:115], 0\n"
      "v_exp_f32 v95, v95\n"
      "v_cvt_pk_bf16_f32 v156, v88, v89\n"
      "v_cvt_pk_bf16_f32 v157, v90, v91\n"
      "v_cvt_pk_bf16_f32 v158, v92, v93\n"
      "v_cvt_pk_bf16_f32 v159, v94, v95\n"
      "v_exp_f32 v96, v96\n"
      "v_exp_f32 v97, v97\n"
      "v_mfma_f32_32x32x16_bf16 v[24:39], v[136:139], v[156:159], v[24:39]\n"
      "v_exp_f32 v98, v98\n"
      "v_exp_f32 v99, v99\n"
      "v_exp_f32 v100, v100\n"
      "v_exp_f32 v101, v101\n"
      "v_exp_f32 v102, v102\n"
      "v_exp_f32 v103, v103\n"
      "v_cvt_pk_bf16_f32 v160, v96, v97\n"
      "v_cvt_pk_bf16_f32 v161, v98, v99\n"
      "v_cvt_pk_bf16_f32 v162, v100, v101\n"
      "v_cvt_pk_bf16_f32 v163, v102, v103\n"
      "s_cmp_lt_u32 %[st], %[sw]\n"
      "s_cbranch_scc1 GMSW%=\n"
      "s_setprio 1\n"
      "GMSW%=:\n"
      "v_exp_f32 v72, v72\n"
      "v_exp_f32 v73, v73\n"
      "v_mfma_f32_32x32x16_bf16 v[24:39], v[140:143], v[160:163], v[24:39]\n"
      "v_exp_f32 v74, v74\n"
      "v_exp_f32 v75, v75\n"
      "v_exp_f32 v76, v76\n"
      "v_exp_f32 v77, v77\n"
      "v_exp_f32 v78, v78\n"
      "v_mfma_f32_32x32x16_bf16 v[88:103], v[132:135], v[116:119], 0\n"
      "v_exp_f32 v79, v79\n"
      "v_cvt_pk_bf16_f32 v156, v72, v73\n"
      "v_cvt_pk_bf16_f32 v157, v74, v75\n"
      "v_cvt_pk_bf16_f32 v158, v76, v77\n"
      "v_cvt_pk_bf16_f32 v159, v78, v79\n"
      "v_exp_f32 v80, v80\n"
      "v_exp_f32 v81, v81\n"
      "v_mfma_f32_32x32x16_bf16 v[40:55], v[136:139], v[156:159], v[40:55]\n"
      "s_add_u32 %[st], %[st], 1\n"
      "v_exp_f32 v82, v82\n"
      "v_exp_f32 v83, v83\n"
      "v_exp_f32 v84, v84\n"
      "v_exp_f32 v85, v85\n"
      "v_exp_f32 v86, v86\n"
      "v_exp_f32 v87, v87\n"
      "v_cvt_pk_bf16_f32 v160, v80, v81\n"
      "v_cvt_pk_bf16_f32 v161, v82, v83\n"
      "v_cvt_pk_bf16_f32 v162, v84, v85\n"
      "v_cvt_pk_bf16_f32 v163, v86, v87\n"
      "v_exp_f32 v88, v88\n"
      "v_exp_f32 v89, v89\n"
      "v_mfma_f32_32x32x16_bf16 v[40:55], v[140:143], v[160:163], v[40:55]\n"
      "s_waitcnt vmcnt(3)\n"
      "v_exp_f32 v90, v90\n"
      "v_exp_f32 v91, v91\n"
      "v_exp_f32 v92, v92\n"
      "v_exp_f32 v93, v93\n"
      "v_exp_f32 v94, v94\n"
      "v_mfma_f32_32x32x16_bf16 v[72:87], v[144:147], v[104:107], 0\n"
      "v_exp_f32 v95, v95\n"
      "v_cvt_pk_bf16_f32 v156, v88, v89\n"
      "v_cvt_pk_bf16_f32 v157, v90, v91\n"
      "v_cvt_pk_bf16_f32 v158, v92, v93\n"
      "v_cvt_pk_bf16_f32 v159, v94, v95\n"
      "v_exp_f32 v96, v96\n"
      "v_exp_f32 v97, v97\n"
      "v_mfma_f32_32x32x16_bf16 v[56:71], v[136:139], v[156:159], v[56:71]\n"
      "v_exp_f32 v98, v98\n"
      "v_exp_f32 v99, v99\n"
      "v_exp_f32 v100, v100\n"
      "v_exp_f32 v101, v101\n"
      "v_exp_f32 v102, v102\n"
      "v_exp_f32 v103, v103\n"
      "v_cvt_pk_bf16_f32 v160, v96, v97\n"
      "v_cvt_pk_bf16_f32 v161, v98, v99\n"
      "v_cvt_pk_bf16_f32 v162, v100, v101\n"
      "v_cvt_pk_bf16_f32 v163, v102, v103\n"
      "v_exp_f32 v72, v72\n"
      "v_exp_f32 v73, v73\n"
      "v_mfma_f32_32x32x16_bf16 v[56:71], v[140:143], v[160:163], v[56:71]\n"
      "s_add_u32 %[so], %[so], 0x2000\n"
      "s_bitset0_b32 %[so], 17\n"
      "v_exp_f32 v74, v74\n"
      "v_exp_f32 v75, v75\n"
      "v_exp_f32 v76, v76\n"
      "v_exp_f32 v77, v77\n"
      "v_exp_f32 v78, v78\n"
      "v_mfma_f32_32x32x16_bf16 v[88:103], v[144:147], v[108:111], 0\n"
      "s_cmp_lg_u32 %[rdy], 0xffff\n"
      "s_cbranch_scc1 GMIC%=\n"
      "GMIRC%=:\n"
      "s_cmp_eq_u32 %[st], 14\n"
      "s_cselect_b32 %[s1], 0x7f000000, %[so]\n"
      "buffer_load_dwordx4 v[132:135], v164, %[srd], %[s1] offen sc1\n"
      "s_mov_b64 exec, %[msk]\n"
      "buffer_load_dwordx4 v[136:139], v164, %[srd], %[s1] offen offset:1024 sc1\n"
      "buffer_load_dwordx4 v[140:143], v164, %[srd], %[s1] offen offset:1280 sc1\n"
      "s_mov_b64 exec, -1\n"
      "v_exp_f32 v79, v79\n"
      "v_cvt_pk_bf16_f32 v156, v72, v73\n"
      "v_cvt_pk_bf16_f32 v157, v74, v75\n"
      "v_cvt_pk_bf16_f32 v158, v76, v77\n"
      "v_cvt_pk_bf16_f32 v159, v78, v79\n"
      "v_exp_f32 v80, v80\n"
      "v_exp_f32 v81, v81\n"
      "v_mfma_f32_32x32x16_bf16 v[8:23], v[148:151], v[156:159], v[8:23]\n"
      "v_exp_f32 v82, v82\n"
      "v_exp_f32 v83, v83\n"
      "v_exp_f32 v84, v84\n"
      "v_exp_f32 v85, v85\n"
      "v_exp_f32 v86, v86\n"
      "v_exp_f32 v87, v87\n"
      "v_cvt_pk_bf16_f32 v160, v80, v81\n"
      "v_cvt_pk_bf16_f32 v161, v82, v83\n"
      "v_cvt_pk_bf16_f32 v162, v84, v85\n"
      "v_cvt_pk_bf16_f32 v163, v86, v87\n"
      "v_exp_f32 v88, v88\n"
      "v_exp_f32 v89, v89\n"
      "v_mfma_f32_32x32x16_bf16 v[8:23], v[152:155], v[160:163], v[8:23]\n"
      "v_exp_f32 v90, v90\n"
      "v_exp_f32 v91, v91\n"
      "v_exp_f32 v92, v92\n"
      "v_exp_f32 v93, v93\n"
      "v_exp_f32 v94, v94\n"
      "v_mfma_f32_32x32x16_bf16 v[72:87], v[144:147], v[112:115], 0\n"
      "v_exp_f32 v95, v95\n"
      "v_cvt_pk_bf16_f32 v156, v88, v89\n"
      "v_cvt_pk_bf16_f32 v157, v90, v91\n"
      "v_cvt_pk_bf16_f32 v158, v92, v93\n"
      "v_cvt_pk_bf16_f32 v159, v94, v95\n"
      "v_exp_f32 v96, v96\n"
      "v_exp_f32 v97, v97\n"
      "v_mfma_f32_32x32x16_bf16 v[24:39], v[148:151], v[156:159], v[24:39]\n"
      "v_exp_f32 v98, v98\n"
      "v_exp_f32 v99, v99\n"
      "v_exp_f32 v100, v100\n"
      "v_exp_f32 v101, v101\n"
      "v_exp_f32 v102, v102\n"
      "v_exp_f32 v103, v103\n"
      "v_cvt_pk_bf16_f32 v160, v96, v97\n"
      "v_cvt_pk_bf16_f32 v161, v98, v99\n"
      "v_cvt_pk_bf16_f32 v162, v100, v101\n"
      "v_cvt_pk_bf16_f32 v163, v102, v103\n"
      "v_exp_f32 v72, v72\n"
      "v_exp_f32 v73, v73\n"
      "v_mfma_f32_32x32x16_bf16 v[24:39], v[152:155], v[160:163], v[24:39]\n"
      "v_exp_f32 v74, v74\n"
      "v_exp_f32 v75, v75\n"
      "v_exp_f32 v76, v76\n"
      "v_exp_f32 v77, v77\n"
      "v_exp_f32 v78, v78\n"
      "v_mfma_f32_32x32x16_bf16 v[88:103], v[144:147], v[116:119], 0\n"
      "v_exp_f32 v79, v79\n"
      "v_cvt_pk_bf16_f32 v156, v72, v73\n"
      "v_cvt_pk_bf16_f32 v157, v74, v75\n"
      "v_cvt_pk_bf16_f32 v158, v76, v77\n"
      "v_cvt_pk_bf16_f32 v159, v78, v79\n"
      "v_exp_f32 v80, v80\n"
      "v_exp_f32 v81, v81\n"
      "v_mfma_f32_32x32x16_bf16 v[40:55], v[148:151], v[156:159], v[40:55]\n"
      "s_add_u32 %[st], %[st], 1\n"
      "v_exp_f32 v82, v82\n"
      "v_exp_f32 v83, v83\n"
      "v_exp_f32 v84, v84\n"
      "v_exp_f32 v85, v85\n"
      "v_exp_f32 v86, v86\n"
      "v_exp_f32 v87, v87\n"
      "v_cvt_pk_bf16_f32 v160, v80, v81\n"
      "v_cvt_pk_bf16_f32 v161, v82, v83\n"
      "v_cvt_pk_bf16_f32 v162, v84, v85\n"
      "v_cvt_pk_bf16_f32 v163, v86, v87\n"
      "v_exp_f32 v88, v88\n"
      "v_exp_f32 v89, v89\n"
      "v_mfma_f32_32x32x16_bf16 v[40:55], v[152:155], v[160:163], v[40:55]\n"
      "s_waitcnt vmcnt(3)\n"
      "v_exp_f32 v90, v90\n"
      "v_exp_f32 v91, v91\n"
      "v_exp_f32 v92, v92\n"
      "v_exp_f32 v93, v93\n"
      "v_exp_f32 v94, v94\n"
      "v_mfma_f32_32x32x16_bf16 v[72:87], v[120:123], v[104:107], 0\n"
      "v_exp_f32 v95, v95\n"
      "v_cvt_pk_bf16_f32 v156, v88, v89\n"
      "v_cvt_pk_bf16_f32 v157, v90, v91\n"
      "v_cvt_pk_bf16_f32 v158, v92, v93\n"
      "v_cvt_pk_bf16_f32 v159, v94, v95\n"
      "v_exp_f32 v96, v96\n"
      "v_exp_f32 v97, v97\n"
      "v_mfma_f32_32x32x16_bf16 v[56:71], v[148:151], v[156:159], v[56:71]\n"
      "v_exp_f32 v98, v98\n"
      "v_exp_f32 v99, v99\n"
      "v_exp_f32 v100, v100\n"
      "v_exp_f32 v101, v101\n"
      "v_exp_f32 v102, v102\n"
      "v_exp_f32 v103, v103\n"
      "v_cvt_pk_bf16_f32 v160, v96, v97\n"
      "v_cvt_pk_bf16_f32 v161, v98, v99\n"
      "v_cvt_pk_bf16_f32 v162, v100, v101\n"
      "v_cvt_pk_bf16_f32 v163, v102, v103\n"
      "s_cmp_lt_u32 %[st], 15\n"
      "s_cbranch_scc1 GMLOOP%=\n"
      "v_exp_f32 v72, v72\n"
      "v_exp_f32 v73, v73\n"
      "v_mfma_f32_32x32x16_bf16 v[56:71], v[152:155], v[160:163], v[56:71]\n"
      "ds_read_b96 v[4:6], v7\n"
      "v_exp_f32 v74, v74\n"
      "v_exp_f32 v75, v75\n"
      "v_exp_f32 v76, v76\n"
      "v_exp_f32 v77, v77\n"
      "v_exp_f32 v78, v78\n"
      "v_mfma_f32_32x32x16_bf16 v[88:103], v[120:123], v[108:111], 0\n"
      "v_exp_f32 v79, v79\n"
      "v_cvt_pk_bf16_f32 v156, v72, v73\n"
      "v_cvt_pk_bf16_f32 v157, v74, v75\n"
      "v_cvt_pk_bf16_f32 v158, v76, v77\n"
      "v_cvt_pk_bf16_f32 v159, v78, v79\n"
      "v_exp_f32 v80, v80\n"
      "v_exp_f32 v81, v81\n"
      "v_mfma_f32_32x32x16_bf16 v[8:23], v[124:127], v[156:159], v[8:23]\n"
      "s_waitcnt vmcnt(0)\n"
      "v_add_u32 v132, %[r0], v164\n"
      "v_add_u32 v133, %[r1], v164\n"
      "v_add_u32 v134, %[r2], v164\n"
      "v_add_u32 v135, %[r3], v164\n"
      "v_mov_b32 v136, %[rs]\n"
      "v_mov_b32 v137, 1\n"
      "v_exp_f32 v82, v82\n"
      "v_exp_f32 v83, v83\n"
      "v_exp_f32 v84, v84\n"
      "v_exp_f32 v85, v85\n"
      "v_exp_f32 v86, v86\n"
      "v_exp_f32 v87, v87\n"
      "v_cvt_pk_bf16_f32 v160, v80, v81\n"
      "v_cvt_pk_bf16_f32 v161, v82, v83\n"
      "v_cvt_pk_bf16_f32 v162, v84, v85\n"
      "v_cvt_pk_bf16_f32 v163, v86, v87\n"
      "v_exp_f32 v88, v88\n"
      "v_exp_f32 v89, v89\n"
      "v_mfma_f32_32x32x16_bf16 v[8:23], v[128:131], v[160:163], v[8:23]\n"
      "v_exp_f32 v90, v90\n"
      "v_exp_f32 v91, v91\n"
      "v_exp_f32 v92, v92\n"
      "v_exp_f32 v93, v93\n"
      "v_exp_f32 v94, v94\n"
      "v_mfma_f32_32x32x16_bf16 v[72:87], v[120:123], v[112:115], 0\n"
      "s_waitcnt lgkmcnt(0)\n"
      "v_mul_f32 v139, v166, v6\n"
      "v_cmp_gt_u32 vcc, 8, v5\n"
      "v_cmp_gt_u32 %[t0], 4, v4\n"
      "s_or_b64 vcc, vcc, %[t0]\n"
      "s_mov_b32 exec_hi, 0\n"
      "v_cmp_nge_f32 %[t0], %[lim], v139\n"
      "s_mov_b32 exec_hi, -1\n"
      "s_or_b64 vcc, vcc, %[t0]\n"
      "s_cmp_lg_u32 %[rdy], 0xffff\n"
      "s_cselect_b64 %[t0], -1, 0\n"
      "s_or_b64 vcc, vcc, %[t0]\n"
      "v_exp_f32 v95, v95\n"
      "v_cvt_pk_bf16_f32 v156, v88, v89\n"
      "v_cvt_pk_bf16_f32 v157, v90, v91\n"
      "v_cvt_pk_bf16_f32 v158, v92, v93\n"
      "v_cvt_pk_bf16_f32 v159, v94, v95\n"
      "v_exp_f32 v96, v96\n"
      "v_exp_f32 v97, v97\n"
      "v_mfma_f32_32x32x16_bf16 v[24:39], v[124:127], v[156:159], v[24:39]\n"
      "s_cbranch_vccnz GMW0_%=\n"
      "ds_write_b128 v132, v[8:11] offset:0\n"
      "ds_write_b128 v132, v[12:15] offset:1024\n"
      "ds_write_b32 v132, v16 offset:6144\n"
      "GMW0_%=:\n"
      "v_exp_f32 v98, v98\n"
      "v_exp_f32 v99, v99\n"
      "v_exp_f32 v100, v100\n"
      "v_exp_f32 v101, v101\n"
      "v_exp_f32 v102, v102\n"
      "v_exp_f32 v103, v103\n"
      "v_cvt_pk_bf16_f32 v160, v96, v97\n"
      "v_cvt_pk_bf16_f32 v161, v98, v99\n"
      "v_cvt_pk_bf16_f32 v162, v100, v101\n"
      "v_cvt_pk_bf16_f32 v163, v102, v103\n"
      "v_exp_f32 v72, v72\n"
      "v_exp_f32 v73, v73\n"
      "v_mfma_f32_32x32x16_bf16 v[24:39], v[128:131], v[160:163], v[24:39]\n"
      "v_exp_f32 v74, v74\n"
      "v_exp_f32 v75, v75\n"
      "v_exp_f32 v76, v76\n"
      "v_exp_f32 v77, v77\n"
      "v_exp_f32 v78, v78\n"
      "v_mfma_f32_32x32x16_bf16 v[88:103], v[120:123], v[116:119], 0\n"
      "v_exp_f32 v79, v79\n"
      "v_cvt_pk_bf16_f32 v156, v72, v73\n"
      "v_cvt_pk_bf16_f32 v157, v74, v75\n"
      "v_cvt_pk_bf16_f32 v158, v76, v77\n"
      "v_cvt_pk_bf16_f32 v159, v78, v79\n"
      "v_exp_f32 v80, v80\n"
      "v_exp_f32 v81, v81\n"
      "v_mfma_f32_32x32x16_bf16 v[40:55], v[124:127], v[156:159], v[40:55]\n"
      "s_cbranch_vccnz GMW1_%=\n"
      "ds_write_b128 v133, v[24:27] offset:2048\n"
      "ds_write_b128 v133, v[28:31] offset:3072\n"
      "ds_write_b32 v133, v32 offset:6148\n"
      "GMW1_%=:\n"
      "v_exp_f32 v82, v82\n"
      "v_exp_f32 v83, v83\n"
      "v_exp_f32 v84, v84\n"
      "v_exp_f32 v85, v85\n"
      "v_exp_f32 v86, v86\n"
      "v_exp_f32 v87, v87\n"
      "v_cvt_pk_bf16_f32 v160, v80, v81\n"
      "v_cvt_pk_bf16_f32 v161, v82, v83\n"
      "v_cvt_pk_bf16_f32 v162, v84, v85\n"
      "v_cvt_pk_bf16_f32 v163, v86, v87\n"
      "v_exp_f32 v88, v88\n"
      "v_exp_f32 v89, v89\n"
      "v_mfma_f32_32x32x16_bf16 v[40:55], v[128:131], v[160:163], v[40:55]\n"
      "v_exp_f32 v90, v90\n"
      "v_exp_f32 v91, v91\n"
      "v_exp_f32 v92, v92\n"
      "v_exp_f32 v93, v93\n"
      "v_exp_f32 v94, v94\n"
      "v_exp_f32 v95, v95\n"
      "v_cvt_pk_bf16_f32 v156, v88, v89\n"
      "v_cvt_pk_bf16_f32 v157, v90, v91\n"
      "v_cvt_pk_bf16_f32 v158, v92, v93\n"
      "v_cvt_pk_bf16_f32 v159, v94, v95\n"
      "v_exp_f32 v96, v96\n"
      "v_exp_f32 v97, v97\n"
      "v_mfma_f32_32x32x16_bf16 v[56:71], v[124:127], v[156:159], v[56:71]\n"
      "s_cbranch_vccnz GMW2_%=\n"
      "ds_write_b128 v134, v[40:43] offset:4096\n"
      "ds_write_b128 v134, v[44:47] offset:5120\n"
      "ds_write_b32 v134, v48 offset:6152\n"
      "s_mov_b64 exec, 1\n"
      "ds_add_u32 v136, v137\n"
      "s_mov_b64 exec, -1\n"
      "GMW2_%=:\n"
      "v_exp_f32 v98, v98\n"
      "v_exp_f32 v99, v99\n"
      "v_exp_f32 v100, v100\n"
      "v_exp_f32 v101, v101\n"
      "v_exp_f32 v102, v102\n"
      "v_exp_f32 v103, v103\n"
      "s_mov_b32 %[st], 0\n"
      "s_setprio 3\n"
      "s_cbranch_vccnz GMNO%=\n"
      "ds_read_b32 v138, v136\n"
      "ds_read_b128 v[8:11], v135 offset:0\n"
      "ds_read_b128 v[12:15], v135 offset:1024\n"
      "ds_read_b128 v[16:19], v135 offset:2048\n"
      "ds_read_b128 v[20:23], v135 offset:3072\n"
      "ds_read_b128 v[24:27], v135 offset:4096\n"
      "ds_read_b128 v[28:31], v135 offset:5120\n"
      "ds_read_b128 v[32:35], v135 offset:6144\n"
      "v_cvt_pk_bf16_f32 v160, v96, v97\n"
      "v_cvt_pk_bf16_f32 v161, v98, v99\n"
      "v_cvt_pk_bf16_f32 v162, v100, v101\n"
      "v_cvt_pk_bf16_f32 v163, v102, v103\n"
      "s_mov_b32 %[st], 1\n"
      "s_mov_b32 %[sp], 0x100000\n"
      "v_mfma_f32_32x32x16_bf16 v[56:71], v[128:131], v[160:163], v[56:71]\n"
      "GMML%=:\n"
      "s_waitcnt lgkmcnt(7)\n"
      "v_readfirstlane_b32 %[s1], v138\n"
      "s_cmp_lg_u32 %[s1], 4\n"
      "s_cbranch_scc1 GMMW%=\n"
      "s_waitcnt lgkmcnt(3)\n"
      "v_pk_add_f32 v[8:9], v[8:9], v[16:17]\n"
      "v_pk_add_f32 v[10:11], v[10:11], v[18:19]\n"
      "v_pk_add_f32 v[12:13], v[12:13], v[20:21]\n"
      "v_pk_add_f32 v[14:15], v[14:15], v[22:23]\n"
      "s_waitcnt lgkmcnt(0)\n"
      "v_pk_add_f32 v[8:9], v[8:9], v[24:25]\n"
      "v_pk_add_f32 v[10:11], v[10:11], v[26:27]\n"
      "v_pk_add_f32 v[12:13], v[12:13], v[28:29]\n"
      "v_pk_add_f32 v[14:15], v[14:15], v[30:31]\n"
      "v_add_f32 v32, v32, v33\n"
      "v_add_f32 v32, v32, v34\n"
      "v_add_f32 v32, v32, v64\n"
      "v_rcp_f32 v32, v32\n"
      "v_pk_add_f32 v[8:9], v[8:9], v[56:57]\n"
      "v_pk_add_f32 v[10:11], v[10:11], v[58:59]\n"
      "v_pk_add_f32 v[12:13], v[12:13], v[60:61]\n"
      "v_pk_add_f32 v[14:15], v[14:15], v[62:63]\n"
      "v_mov_b32 v136, %[ad]\n"
      "v_pk_mul_f32 v[8:9], v[8:9], v[32:33] op_sel_hi:[1,0]\n"
      "v_pk_mul_f32 v[10:11], v[10:11], v[32:33] op_sel_hi:[1,0]\n"
      "v_pk_mul_f32 v[12:13], v[12:13], v[32:33] op_sel_hi:[1,0]\n"
      "v_pk_mul_f32 v[14:15], v[14:15], v[32:33] op_sel_hi:[1,0]\n"
      "v_cvt_pk_bf16_f32 v8, v8, v9\n"
      "v_cvt_pk_bf16_f32 v9, v10, v11\n"
      "v_cvt_pk_bf16_f32 v10, v12, v13\n"
      "v_cvt_pk_bf16_f32 v11, v14, v15\n"
      "buffer_store_dwordx4 v[8:11], v164, %[srd], %[oo] offen sc1\n"
      "s_mov_b64 exec, 1\n"
      "ds_add_u32 v136, v137\n"
      "s_endpgm\n"
      "GMMW%=:\n"
      "s_cmp_gt_u32 %[s1], 4\n"
      "s_cbranch_scc1 GMOUT%=\n"
      "s_sub_u32 %[sp], %[sp], 1\n"
      "s_cbranch_scc1 GMOUT%=\n"
      "s_sleep 1\n"
      "ds_read_b32 v138, v136\n"
      "s_waitcnt lgkmcnt(0)\n"
      "v_readfirstlane_b32 %[s1], v138\n"
      "s_cmp_lg_u32 %[s1], 4\n"
      "s_cbranch_scc1 GMMW%=\n"
      "ds_read_b32 v138, v136\n"
      "ds_read_b128 v[8:11], v135 offset:0\n"
      "ds_read_b128 v[12:15], v135 offset:1024\n"
      "ds_read_b128 v[16:19], v135 offset:2048\n"
      "ds_read_b128 v[20:23], v135 offset:3072\n"
      "ds_read_b128 v[24:27], v135 offset:4096\n"
      "ds_read_b128 v[28:31], v135 offset:5120\n"
      "ds_read_b128 v[32:35], v135 offset:6144\n"
      "s_branch GMML%=\n"
      "GMNO%=:\n"
      "v_cvt_pk_bf16_f32 v160, v96, v97\n"
      "v_cvt_pk_bf16_f32 v161, v98, v99\n"
      "v_cvt_pk_bf16_f32 v162, v100, v101\n"
      "v_cvt_pk_bf16_f32 v163, v102, v103\n"
      "s_nop 1\n"
      "v_mfma_f32_32x32x16_bf16 v[56:71], v[128:131], v[160:163], v[56:71]\n"
      "s_nop 7\n"
      "s_branch GMOUT%=\n"
      "GMIZ%=:\n"
      "s_bfe_u32 %[s1], %[so], 0x4000d\n"
      "s_mov_b32 %[sp], 0x10000\n"
      "GMIZL%=:\n"
      "v_cmp_eq_u32 vcc, %[tag], v167\n"
      "s_nop 0\n"
      "s_lshr_b32 %[rdy], vcc_lo, 16\n"
      "s_and_b32 %[rdy], %[rdy], vcc_lo\n"
      "s_bitcmp1_b32 %[rdy], %[s1]\n"
      "s_cbranch_scc1 GMIZO%=\n"
      "s_sub_u32 %[sp], %[sp], 1\n"
      "s_cbranch_scc1 GMIZO%=\n"
      "s_sleep 0\n"
      "s_not_b32 exec_lo, vcc_lo\n"
      "s_mov_b32 exec_hi, 0\n"
      "buffer_load_dwordx2 v[166:167], v165, %[srd], %[flb] offen sc1\n"
      "s_mov_b64 exec, -1\n"
      "s_waitcnt vmcnt(0)\n"
      "s_branch GMIZL%=\n"
      "GMIZO%=:\n"
      "s_cmp_eq_u32 %[rdy], 0xffff\n"
      "s_cbranch_scc1 GMIRZ%=\n"
      "s_not_b32 exec_lo, vcc_lo\n"
      "s_mov_b32 exec_hi, 0\n"
      "buffer_load_dwordx2 v[166:167], v165, %[srd], %[flb] offen sc1\n"
      "s_mov_b64 exec, -1\n"
      "s_branch GMIRZ%=\n"
      "GMIA%=:\n"
      "s_bfe_u32 %[s1], %[so], 0x4000d\n"
      "s_mov_b32 %[sp], 0x10000\n"
      "GMIAL%=:\n"
      "v_cmp_eq_u32 vcc, %[tag], v167\n"
      "s_nop 0\n"
      "s_lshr_b32 %[rdy], vcc_lo, 16\n"
      "s_and_b32 %[rdy], %[rdy], vcc_lo\n"
      "s_bitcmp1_b32 %[rdy], %[s1]\n"
      "s_cbranch_scc1 GMIAO%=\n"
      "s_sub_u32 %[sp], %[sp], 1\n"
      "s_cbranch_scc1 GMIAO%=\n"
      "s_sleep 0\n"
      "s_not_b32 exec_lo, vcc_lo\n"
      "s_mov_b32 exec_hi, 0\n"
      "buffer_load_dwordx2 v[166:167], v165, %[srd], %[flb] offen sc1\n"
      "s_mov_b64 exec, -1\n"
      "s_waitcnt vmcnt(0)\n"
      "s_branch GMIAL%=\n"
      "GMIAO%=:\n"
      "s_cmp_eq_u32 %[rdy], 0xffff\n"
      "s_cbranch_scc1 GMIRA%=\n"
      "s_not_b32 exec_lo, vcc_lo\n"
      "s_mov_b32 exec_hi, 0\n"
      "buffer_load_dwordx2 v[166:167], v165, %[srd], %[flb] offen sc1\n"
      "s_mov_b64 exec, -1\n"
      "s_branch GMIRA%=\n"
      "GMIB%=:\n"
      "s_bfe_u32 %[s1], %[so], 0x4000d\n"
      "s_mov_b32 %[sp], 0x10000\n"
      "GMIBL%=:\n"
      "v_cmp_eq_u32 vcc, %[tag], v167\n"
      "s_nop 0\n"
      "s_lshr_b32 %[rdy], vcc_lo, 16\n"
      "s_and_b32 %[rdy], %[rdy], vcc_lo\n"
      "s_bitcmp1_b32 %[rdy], %[s1]\n"
      "s_cbranch_scc1 GMIBO%=\n"
      "s_sub_u32 %[sp], %[sp], 1\n"
      "s_cbranch_scc1 GMIBO%=\n"
      "s_sleep 0\n"
      "s_not_b32 exec_lo, vcc_lo\n"
      "s_mov_b32 exec_hi, 0\n"
      "buffer_load_dwordx2 v[166:167], v165, %[srd], %[flb] offen sc1\n"
      "s_mov_b64 exec, -1\n"
      "s_waitcnt vmcnt(0)\n"
      "s_branch GMIBL%=\n"
      "GMIBO%=:\n"
      "s_cmp_eq_u32 %[rdy], 0xffff\n"
      "s_cbranch_scc1 GMIRB%=\n"
      "s_not_b32 exec_lo, vcc_lo\n"
      "s_mov_b32 exec_hi, 0\n"
      "buffer_load_dwordx2 v[166:167], v165, %[srd], %[flb] offen sc1\n"
      "s_mov_b64 exec, -1\n"
      "s_branch GMIRB%=\n"
      "GMIC%=:\n"
      "s_bfe_u32 %[s1], %[so], 0x4000d\n"
      "s_mov_b32 %[sp], 0x10000\n"
      "GMICL%=:\n"
      "v_cmp_eq_u32 vcc, %[tag], v167\n"
      "s_nop 0\n"
      "s_lshr_b32 %[rdy], vcc_lo, 16\n"
      "s_and_b32 %[rdy], %[rdy], vcc_lo\n"
      "s_bitcmp1_b32 %[rdy], %[s1]\n"
      "s_cbranch_scc1 GMICO%=\n"
      "s_sub_u32 %[sp], %[sp], 1\n"
      "s_cbranch_scc1 GMICO%=\n"
      "s_sleep 0\n"
      "s_not_b32 exec_lo, vcc_lo\n"
      "s_mov_b32 exec_hi, 0\n"
      "buffer_load_dwordx2 v[166:167], v165, %[srd], %[flb] offen sc1\n"
      "s_mov_b64 exec, -1\n"
      "s_waitcnt vmcnt(0)\n"
      "s_branch GMICL%=\n"
      "GMICO%=:\n"
      "s_cmp_eq_u32 %[rdy], 0xffff\n"
      "s_cbranch_scc1 GMIRC%=\n"
      "s_not_b32 exec_lo, vcc_lo\n"
      "s_mov_b32 exec_hi, 0\n"
      "buffer_load_dwordx2 v[166:167], v165, %[srd], %[flb] offen sc1\n"
      "s_mov_b64 exec, -1\n"
      "s_branch GMIRC%=\n"
      ".Lgmend:\n"
      "GMOUT%=:\n"
      "s_waitcnt vmcnt(0) lgkmcnt(0)\n"
      "s_nop 7\n"
          : "={v[8:23]}"(O0), "={v[24:39]}"(O1), "={v[40:55]}"(O2), "={v[56:71]}"(O3), "={v[120:123]}"(KA), "={v[124:127]}"(VA0),
            "={v[128:131]}"(VA1), "={v[104:107]}"(Q0), "={v[108:111]}"(Q1), "={v[112:115]}"(Q2), "={v[116:119]}"(Q3),
            "+{v[166:167]}"(fl), "+{v[4:7]}"(misc), [so] "+s"(so), [s1] "=&s"(s1), [st] "=&s"(st), [rdy] "=&s"(rdy),
            [sp] "=&s"(sp), [t0] "=&s"(t0)
          : "{v42}"(a42), "{v43}"(a43), "{v44}"(a44), "{v45}"(a45), "{v164}"(vof), "{v165}"(flo),
            [srd] "s"(sws), [msk] "s"(0x0000ffff0000ffffull), [tag] "s"(tag), [flb] "s"(flb), [sw] "i"(13), [hi] "s"(hd),
            [lim] "s"(SAFE2), [r0] "s"(RADDR(tb)), [r1] "s"(RADDR((tb + 1) & 3)), [r2] "s"(RADDR((tb + 2) & 3)), [r3] "s"(RADDR((tb + 3) & 3)),
            [rs] "s"(M_RSYNC + hd * 4), [ad] "s"(M_ADONE + hd * 4), [oo] "s"(WS_O + ((4 * t + ((tb + 3) & 3)) * 32 + head) * 1024)
          : "memory", "scc", "vcc",
            "v72", "v73", "v74", "v75", "v76", "v77", "v78", "v79", "v80", "v81", "v82", "v83", "v84", "v85", "v86", "v87", "v88", "v89", "v90", "v91", "v92", "v93", "v94", "v95", "v96", "v97", "v98", "v99", "v100", "v101", "v102", "v103", "v132", "v133", "v134", "v135", "v136", "v137", "v138", "v139", "v140", "v141", "v142", "v143", "v144", "v145", "v146", "v147", "v148", "v149", "v150", "v151", "v152", "v153", "v154", "v155", "v156", "v157", "v158", "v159", "v160", "v161", "v162", "v163");
    }
    {
    const int lane = fresh_lane(), r = lane & 31, hh = lane >> 5;
    const unsigned l16 = lane * 16;
    if (st < 2) {
    float m0 = 0.f, m1 = 0.f, m2 = 0.f, m3 = 0.f;
    if (st == 0) {
      unsigned inc = 1u;
#pragma unroll 1
      for (int i = 0; i < SPIN_MAX && __any(fl[1] != tag); ++i)
        asm volatile("buffer_load_dwordx2 %0, %1, %2, %3 offen sc1\n\ts_waitcnt vmcnt(0)" : "=&v"(fl) : "v"(r * 128 + hd * 8), "s"(sws), "s"(flb) : "memory");
      lwait<1>(M_HD + hd * 16 + 4, 8);
      const float qm = *lp<float>(M_HD + hd * 16 + 8);
      if (!__all(qm * asf(fl[0]) <= SAFE2)) {
        inc = 1u + (0x100u << tb);
        m0 = m1 = m2 = m3 = -3.0e38f;
#pragma unroll
        for (int i = 0; i < 16; ++i) { O0[i] = 0.f; O1[i] = 0.f; O2[i] = 0.f; O3[i] = 0.f; }
        const f32x16 z = {0, 0, 0, 0, 0, 0, 0, 0, 0, 0, 0, 0, 0, 0, 0, 0};
#pragma unroll 1
        for (int ss = 0; ss < 16; ++ss) {
          kv_load(KA, VA0, VA1, sws, l16, kvh + ss * 8192);
          asm volatile("s_waitcnt vmcnt(0)" : "+v"(KA), "+v"(VA0), "+v"(VA1)::"memory");
#define SLOW(Q, O, m)                                                                 \
  {                                                                                   \
    f32x16 S = MFMA(KA, Q, z);                                                        \
    float tm = S[0];                                                                  \
    _Pragma("unroll") for (int i = 1; i < 16; ++i) tm = fmaxf(tm, S[i]);              \
    tm = fmaxf(tm, xhalf(tm, lane));                                                        \
    const float mn = fmaxf(m, tm), al = __builtin_amdgcn_exp2f(m - mn);               \
    m = mn;                                                                           \
    _Pragma("unroll") for (int i = 0; i < 16; ++i) { O[i] *= al; S[i] = __builtin_amdgcn_exp2f(S[i] - mn); } \
    O = MFMA(VA0, pack8<0>(S), O);                                                    \
    O = MFMA(VA1, pack8<8>(S), O);                                                    \
  }
          SLOW(Q0, O0, m0) SLOW(Q1, O1, m1) SLOW(Q2, O2, m2) SLOW(Q3, O3, m3)
        }
      }
      lwait<2>(M_HD, 4);
#define RWR(O, m, j)                                                                                              \
  {                                                                                                               \
    const unsigned a = RADDR((tb + j) & 3) + l16;                                                                 \
    f32x4 lo = {O[0], O[1], O[2], O[3]}, hi = {O[4], O[5], O[6], O[7]};                                           \
    *lp<f32x4>(a + j * 2048) = lo;                                                                                \
    *lp<f32x4>(a + j * 2048 + 1024) = hi;                                                                         \
    *lp<float>(a + 6144 + 4 * j) = (hh && inc > 1u) ? m : O[8];                                                   \
  }
      RWR(O0, m0, 0) RWR(O1, m1, 1) RWR(O2, m2, 2)
      larrive(M_RSYNC + hd * 4, inc);
    }
    unsigned cnt = 0;
#pragma unroll 1
    for (int i = 0; i < (SPIN_MAX << 4) && (cnt & 0xff) < 4; ++i) { cnt = lword(M_RSYNC + hd * 4); __builtin_amdgcn_s_sleep(1); }
    asm volatile("" ::: "memory");
    const unsigned ra = RADDR((tb + 3) & 3);
    const f32x4 lv = *lp<f32x4>(ra + 6144 + r * 16), mv = *lp<f32x4>(ra + 6144 + (32 + r) * 16);
    float mj[3], mm = m3, o[8], l = 0.f;
#pragma unroll
    for (int j = 0; j < 3; ++j) { mj[j] = ((cnt >> (8 + ((tb + 3 - j) & 3))) & 1) ? mv[j] : 0.f; mm = fmaxf(mm, mj[j]); }
#pragma unroll
    for (int j = 0; j < 4; ++j) {
      const float aj = __builtin_amdgcn_exp2f((j < 3 ? mj[j] : m3) - mm);
      f32x4 lo = {O3[0], O3[1], O3[2], O3[3]}, hi = {O3[4], O3[5], O3[6], O3[7]};
      if (j < 3) { lo = *lp<f32x4>(ra + j * 2048 + l16); hi = *lp<f32x4>(ra + j * 2048 + 1024 + l16); }
      l = __builtin_fmaf(aj, j < 3 ? lv[j] : O3[8], l);
#pragma unroll
      for (int i = 0; i < 4; ++i) { o[i] = __builtin_fmaf(aj, lo[i], j ? o[i] : 0.f); o[4 + i] = __builtin_fmaf(aj, hi[i], j ? o[4 + i] : 0.f); }
    }
    const float il = __builtin_amdgcn_rcpf(l);
    const u32x4 ov = {pk2(o[0] * il, o[1] * il), pk2(o[2] * il, o[3] * il), pk2(o[4] * il, o[5] * il), pk2(o[6] * il, o[7] * il)};
    st16c(ov, sws, l16, WS_O + ((4 * t + ((tb + 3) & 3)) * 32 + head) * 1024);
    larrive(M_ADONE + hd * 4);
    }
    }
  } else {
    const int lane = fresh_lane(), r = lane & 31, hh = lane >> 5;
    const unsigned l16 = lane * 16;
    const unsigned xbase = r * 128 + ((hh ^ ((r >> 1) & 7)) << 4);
    const int tq = (lane & 15) >> 2, tp = lane & 3, tblk = (lane >> 4) & 1;
    const int m = w;
    const int q = p == 7 ? 14 : p - (p > 7 && p < 15);
    const int rt = xcd * 4 + (idx >> 3), ct = idx & 7;
    PRIO(3);
    const u32x4 s1 = make_srd(w1, 512 * 1792 * 2);
    const unsigned v1o = (lane >> 2) * 3584 + (lane & 3) * 16;
    const unsigned s1o = q < 14 ? (128 * q + 32 * m) * 2 : OOB;
    unsigned k1 = (kc0 & 7) * 4;
#define W1OFF(ks) (s1o + k1 * 57344)
#define W1NEXT() { k1 = (k1 + 1) & 31; asm volatile("" : "+s"(k1)); }
    u32x4 w1r[6];
    u32x2 b1r[4];
    unsigned tag;
    {
      const u32x4 sx = make_srd(x, 2048 * 512 * 2), sw = make_srd(wqkv, 512 * 1536 * 2);
      const unsigned vE = (lane >> 3) * 1024 + (((lane & 7) ^ (lane >> 4)) << 4), vO = vE ^ 64;
      const unsigned vW = (lane >> 2) * 3072 + (lane & 3) * 16;
      const unsigned xo = (t * 128 + 32 * m) * 1024, wof = 16 * m * 3072 + p * 64;
#define ISSUEX(i)                                                                                       \
  _Pragma("unroll") for (int j = 0; j < 4; ++j)                                                         \
      dma16(sx, (j & 1) ? vO : vE, xo + j * 8192 + ((kc0 + (i)) & 7) * 128, L_X + (i) * 16384 + m * 4096 + j * 1024);
#define WDMA(i, seg, dst) dma16(sw, vW, wof + ((kc0 + (i)) & 7) * 196608 + (seg) * 1024, L_WR + (dst) + m * 1024);
      const unsigned vK = (lane >> 3) * 3072 + (((lane & 7) ^ (((lane >> 4) & 1) << 2)) << 4);
      const unsigned kof = 16 * m * 3072 + (1 + (p & 1)) * 1024 + (p >> 1) * 128;
#define ISSUEW(i) _Pragma("unroll") for (int j = 0; j < 2; ++j)                                         \
      dma16(sw, vK, kof + ((kc0 + (i)) & 7) * 196608 + j * 24576, L_WR + ((i) % 3) * 8192 + m * 2048 + j * 1024);
#define ISSUEQ(j, base) WDMA(2 * (j), 0, base) WDMA(2 * (j) + 1, 0, (base) + 4096)
      ISSUEW(0) ISSUEX(0)
#pragma unroll
      for (int i = 0; i < 8; ++i) {
        if (i < 7) { ISSUEW(i + 1) ISSUEX(i + 1) }
        if (i == 6) { ISSUEQ(0, 24576) }
        if (i == 7) { ISSUEQ(1, 16384) }
        asm volatile("s_waitcnt vmcnt(%0)" ::"i"(i == 7 ? 4 : i == 6 ? 8 : 6) : "memory");
        bar();
      }
      tag = load_tag(ws);
      VMCNT(0);
      bar();
      ISSUEQ(2, 0) ISSUEQ(3, 8192)
      VMCNT(0);
      bar();
#pragma unroll
      for (int j = 0; j < 6; ++j) { ld16(w1r[j], s1, v1o, W1OFF(j)); W1NEXT() }
      if (q == 14) ld16(w1r[0], make_srd(gw, 1024), l16, 0);
      const u32x4 sb = make_srd(b1, 1792 * 2);
#pragma unroll
      for (int q = 0; q < 4; ++q) asm volatile("buffer_load_dwordx2 %0, %1, %2, %3 offen" : "=v"(b1r[q]) : "v"(hh * 8 + q * 16), "s"(sb), "s"(s1o) : "memory");
    }
    if (m == 0 && lane < 32) *lp<unsigned>(L_MISC + lane * 4) = 0u;
    bar();
    bar();
    asm volatile("s_waitcnt vmcnt(0)" : "+v"(w1r[0]), "+v"(w1r[1]), "+v"(w1r[2]), "+v"(w1r[3]), "+v"(w1r[4]), "+v"(w1r[5]), "+v"(b1r[0]), "+v"(b1r[1]), "+v"(b1r[2]), "+v"(b1r[3])::"memory");
    u32x4 sent = {0xffffffffu, 0xffffffffu, 0xffffffffu, 0xffffffffu};
    asm volatile("" : "+v"(sent));
#define OARM() { st16c(sent, sws, l16, WS_O + ((4 * t + m) * 32 + 2 * p) * 1024); st16c(sent, sws, l16, WS_O + ((4 * t + m) * 32 + 2 * p + 1) * 1024); }
    if (q < 14) {
      const unsigned ring = L_W1 + m * 7168;
#define W1DMA(ks) { dma16(s1, v1o, W1OFF(ks), ring + ((ks) % 7) * 1024); W1NEXT() }
#pragma unroll
      for (int j = 0; j < 6; ++j) lwr16(ring + j * 1024 + l16, w1r[j]);
      f32x16 a0, a1, a2, a3;
      {
#pragma unroll
        for (int q = 0; q < 4; ++q) {
          u32x2 v = b1r[q];
          a0[4 * q] = bflo(v[0]); a0[4 * q + 1] = bfhi(v[0]); a0[4 * q + 2] = bflo(v[1]); a0[4 * q + 3] = bfhi(v[1]);
        }
        a1 = a0; a2 = a0; a3 = a0;
      }
      unsigned xa[2][4], wa = ring + (8 * hh + tq) * 64 + tblk * 32 + tp * 8;
#pragma unroll
      for (int s = 0; s < 4; ++s) { xa[0][s] = L_X + (xbase ^ (s << 5)); xa[1][s] = xa[0][s] + 65536; asm volatile("" : "+v"(xa[0][s]), "+v"(xa[1][s])); }
      asm volatile("" : "+v"(wa));
      u32x4 fw[2], fx[2][4];
#define HREAD(ks)                                                                                   \
  {                                                                                                 \
    fw[(ks) & 1] = ltr(wa + ((ks) % 7) * 1024, 256);                                                \
    _Pragma("unroll") for (int j = 0; j < 4; ++j)                                                   \
        fx[(ks) & 1][j] = lrd16(xa[(ks) >> 4][(ks) & 3] + (((ks) >> 2) & 3) * 16384 + j * 4096);    \
  }
      HREAD(0)
      SB;
      PRIO(0);
#pragma unroll
      for (int ks = 0; ks < 32; ++ks) {
        const int n = ks + 1;
#define HX(j) if (n < 32) fx[n & 1][j] = lrd16(xa[n >> 4][n & 3] + ((n >> 2) & 3) * 16384 + j * 4096);
        a0 = MFMA(fw[ks & 1], fx[ks & 1][0], a0);
        SB;
        if (n < 32) { asm volatile("s_waitcnt vmcnt(%0)" ::"i"(30 - ks < 4 ? 30 - ks : 4) : "memory"); fw[n & 1] = ltr(wa + (n % 7) * 1024, 256); }
        HX(0)
        SB;
        a1 = MFMA(fw[ks & 1], fx[ks & 1][1], a1);
        SB;
        HX(1) HX(2) HX(3)
        SB;
        a2 = MFMA(fw[ks & 1], fx[ks & 1][2], a2);
        SB;
        if (ks + 6 < 32) W1DMA(ks + 6)
        SB;
        a3 = MFMA(fw[ks & 1], fx[ks & 1][3], a3);
        SB;
        if (ks == 14) OARM()
        SB;
      }
      PRIO(3);
      larrive(M_MSYNC);
#define HOUT(a, tbb)                                                                                                          \
  {                                                                                                                           \
    _Pragma("unroll") for (int i = 0; i < 16; ++i) { float v = __builtin_amdgcn_fmed3f(a[i], 0.f, __builtin_inff()); a[i] = v * v; } \
    const unsigned o = WS_H + ((4 * t + tbb) * 112 + 8 * q + 2 * m) * 1024;                                                   \
    st16c(pack8<0>(a), sws, l16, o);                                                                                          \
    st16c(pack8<8>(a), sws, l16, o + 1024);                                                                                   \
  }
      HOUT(a0, 0) HOUT(a1, 1) HOUT(a2, 2) HOUT(a3, 3)
    } else if (q == 14) {
      f32x16 a;
#pragma unroll
      for (int i = 0; i < 16; ++i) a[i] = 0.f;
      lwr16(L_W1 + m * 7168 + l16, w1r[0]);
#pragma unroll 1
      for (int c = 0; c < 8; ++c) {
#pragma unroll
        for (int s = 0; s < 4; ++s) {
          u32x4 gf = lrd16(L_W1 + m * 7168 + (4 * ((kc0 + c) & 7) + s) * 32 + hh * 16);
          u32x4 xf = lrd16(L_X + (xbase ^ (s << 5)) + c * 16384 + m * 4096);
          a = MFMA(gf, xf, a);
        }
      }
      larrive(M_MSYNC);
      const float g = __builtin_amdgcn_rcpf(1.f + __builtin_amdgcn_exp2f(-1.4426950408889634f * a[0]));
      if (lane < 32) st4c(asu(g), sws, lane * 4, WS_G + (128 * t + 32 * m) * 4);
    } else {
      larrive(M_MSYNC);
    }
    if (q >= 14) OARM()
    VMCNT(0);
    if (lane < 32) st4c(tag, sws, lane * 4, WS_HF + (t * 64 + 4 * q + m) * 128);
    larrive(M_HD); larrive(M_HD + 16);
    if (q >= 14) __builtin_amdgcn_s_sleep(20);
    lwait<2>(M_MSYNC, 4);

    const int rb = m >> 1, cb = m & 1;
    const unsigned dvo = (lane >> 3) * 1024 + (((lane & 7) ^ (((lane >> 4) & 1) << 2)) << 4);
    const unsigned trp0 = (4 * hh + tq) * 128 + ((cb ^ (tq >> 1)) << 6) + tblk * 32 + tp * 8;
    f32x16 c0, c1, c2, c3;
    const unsigned rg = L_RG + m * RGN;
    unsigned gv, hfv;
    {
      const u32x4 s2 = make_srd(w2, 1792 * 512 * 2), so_ = make_srd(wo, 512 * 512 * 2);
      const unsigned so = 28 * m * 16384 + ct * 128;
      int kk = 7 * (rt & 3) + ((7 * ct) >> 1);
      kk = kk >= 28 ? kk - 28 : kk;
      int kw = kk;
#define SLOT(s) (((s) + 8) % 12)
#define W2DMA(sl) { dma16(s2, dvo, so + kw * 16384, rg + (sl) * 2048); dma16(s2, dvo, so + kw * 16384 + 8192, rg + (sl) * 2048 + 1024); \
                    kw = kw == 27 ? 0 : kw + 1; }
#pragma unroll 1
      for (int j = 0; j < PD; ++j) { W2DMA(SLOT(j)) __builtin_amdgcn_s_sleep(5); }
      {
        const u32x2* bp = (const u32x2*)(b2 + 64 * ct + 32 * cb + 4 * hh);
#pragma unroll
        for (int q = 0; q < 4; ++q) {
          u32x2 v = bp[2 * q];
          c0[4 * q] = bflo(v[0]); c0[4 * q + 1] = bfhi(v[0]); c0[4 * q + 2] = bflo(v[1]); c0[4 * q + 3] = bfhi(v[1]);
        }
      }
      {
        const unsigned fo = (lane < 14 ? 14 * m + lane : 56 + 2 * (rt & 1) + rb) * 128;
#pragma unroll 1
        for (int i = 0; i < SPIN_MAX; ++i) {
          if (__all(ld4c_wait(sws, fo, WS_HF + (rt >> 1) * 8192) == tag)) break;
          __builtin_amdgcn_s_sleep(6);
        }
      }
      const unsigned h0 = WS_H + ((2 * rt + rb) * 112 + 28 * m) * 1024, h1 = WS_H + ((2 * rt + (rb ^ 1)) * 112 + 28 * m) * 1024;
      u32x4 ha[7], hb[7], f0[2], f1[2];
      ld4c(gv, sws, r * 4, WS_G + (64 * rt + 32 * rb) * 4);
#pragma unroll
      for (int j = 0; j < 6; ++j) {
        ld16c(ha[j], sws, l16, h0 + kk * 1024); ld16c(hb[j], sws, l16, h1 + kk * 1024);
        kk = kk == 27 ? 0 : kk + 1;
      }
      unsigned wa0 = rg + trp0, wa1 = rg + (trp0 ^ 64), hfo = ((lane & 15) * 4 + 2 * (rt & 1) + rb) * 128;
      asm volatile("" : "+v"(wa0), "+v"(wa1), "+v"(hfo));
      f0[0] = ltr(wa0 + SLOT(0) * 2048, 1024); f1[0] = ltr(wa1 + SLOT(0) * 2048, 1024);
      SB;
      PRIO(0);
#define WO0 (29 - PD)
#define NISS(i) (((i) >= 1 && (i) < WO0 + 8 ? 2 : 0) + ((i) >= 0 && (i) < 22 ? 2 : 0) + ((i) == 22 ? 1 : 0))
      asm volatile("s_waitcnt vmcnt(10)" : "+v"(ha[0]), "+v"(hb[0])::"memory");
#pragma unroll
      for (int ks = 0; ks < 28; ++ks) {
        const int j = ks % 7, jn = (ks + 6) % 7, n = ks + 1;
#define BDMA(h)                                                                                                              \
  if (ks >= 1 && ks < WO0) dma16(s2, dvo, so + kw * 16384 + h * 8192, rg + SLOT(ks + PD - 1) * 2048 + h * 1024);             \
  if (ks >= WO0 && ks < WO0 + 8) dma16(so_, dvo, (128 * m + 16 * (ks - WO0) + 8 * h) * 1024 + ct * 128, rg + (ks - WO0) * 2048 + h * 1024);
        c0 = MFMA(f0[ks & 1], ha[j], c0);
        SB;
        if (n < 28) f0[n & 1] = ltr(wa0 + SLOT(n) * 2048, 1024);
        BDMA(0)
        SB;
        c1 = MFMA(f1[ks & 1], ha[j], ks ? c1 : zero16());
        SB;
        if (n < 28) f1[n & 1] = ltr(wa1 + SLOT(n) * 2048, 1024);
        BDMA(1)
        if (ks >= 1 && ks < WO0) kw = kw == 27 ? 0 : kw + 1;
        SB;
        c2 = MFMA(f0[ks & 1], hb[j], ks ? c2 : zero16());
        SB;
        if (ks == 22) ld4c(hfv, sws, hfo, WS_HF + (rt >> 1) * 8192);
        if (ks + 6 < 28) ld16c(ha[jn], sws, l16, h0 + kk * 1024);
        SB;
        c3 = MFMA(f1[ks & 1], hb[j], ks ? c3 : zero16());
        SB;
        if (ks + 6 < 28) { ld16c(hb[jn], sws, l16, h1 + kk * 1024); kk = kk == 27 ? 0 : kk + 1; }
        if (n < 28) {
          const int nh = (n < 6 ? (5 - n) * 2 : 0) + NISS(n - 1) + NISS(n - 2) + NISS(n - 3) + NISS(n - 4) + NISS(n - 5);
          asm volatile("s_waitcnt vmcnt(%2)" : "+v"(ha[n % 7]), "+v"(hb[n % 7]) : "i"(nh) : "memory");
        }
        SB;
      }
      PRIO(3);
      asm volatile("s_waitcnt vmcnt(0)" : "+v"(gv), "+v"(hfv)::"memory");
#pragma unroll 1
      for (int i = 0; i < SPIN_MAX && !__all(hfv == tag); ++i) hfv = ld4c_wait(sws, hfo, WS_HF + (rt >> 1) * 8192);
    }
    const float g = asf(gv);
    const unsigned xme = rg + 16384 + l16;
#define XWR(c, o)                                                                                  \
  _Pragma("unroll") for (int q = 0; q < 4; ++q) {                                                  \
    f32x4 v = {c[4 * q], c[4 * q + 1], c[4 * q + 2], c[4 * q + 3]};                                \
    *lp<f32x4>(xme + o + q * 1024) = v;                                                            \
  }
#define XADD(c, a)                                                                                 \
  _Pragma("unroll") for (int q = 0; q < 4; ++q) {                                                  \
    f32x4 v = *lp<f32x4>(a + q * 1024);                                                            \
    c[4 * q] += v[0]; c[4 * q + 1] += v[1]; c[4 * q + 2] += v[2]; c[4 * q + 3] += v[3];            \
  }
    XWR(c2, 0) XWR(c3, 4096)
    larrive(M_MSYNC);
    lwait<1>(M_MSYNC, 8);
    XADD(c0, L_RG + (m ^ 2) * RGN + 16384 + l16) XADD(c1, L_RG + (m ^ 2) * RGN + 16384 + 4096 + l16)
    larrive(M_MSYNC);
#pragma unroll
    for (int i = 0; i < 16; ++i) { c0[i] *= 1.f - g; c1[i] *= 1.f - g; }
    asm volatile("" : "+v"(c0), "+v"(c1));
    lwait<1>(M_MSYNC, 12);
    {
      u32x4 of[8], f[3], e[3];
      const unsigned ob = WS_O + ((2 * rt + rb) * 32 + 16 * cb) * 1024;
      unsigned wb0 = L_RG + 2 * cb * RGN + trp0, wb1 = L_RG + 2 * cb * RGN + (trp0 ^ 64);
      asm volatile("" : "+v"(wb0), "+v"(wb1));
#define LAG 5
#define HJ(j) (((j) & 7) * 2 + ((j) < 8 ? 1 : 0))
#define WOA(j) ((HJ(j) >> 3) * RGN + (HJ(j) & 7) * 2048)
#define C2STEP(j) { c2 = MFMA(e[(j) % 3], of[(j) & 7], (j) ? c2 : zero16()); SB; if ((j) + 3 < 16) e[(j) % 3] = ltr(wb0 + WOA((j) + 3), 1024); }
#define OLD(u) asm volatile("buffer_load_dwordx4 %0, %1, %2, %3 offen sc1" : "+v"(of[(u) & 7]) : "v"(l16), "s"(sws), "s"(ob + HJ(u) * 1024) : "memory");
#pragma unroll
      for (int j = 0; j < 3; ++j) { f[j] = ltr(wb1 + WOA(j), 1024); e[j] = ltr(wb0 + WOA(j), 1024); }
#pragma unroll
      for (int j = 0; j < 16; ++j) {
        const int je = (j | 7) + 1;
        SB;
        if ((j & 7) == 0) {
          lwait<3>(M_ADONE + (j ? 0 : 4), 4);
#pragma unroll
          for (int u = j; u < je; ++u) ld16c(of[u & 7], sws, l16, ob + HJ(u) * 1024);
        }
        asm volatile("s_waitcnt vmcnt(%1)" : "+v"(of[j & 7]) : "i"(je - 1 - j) : "memory");
        if (__builtin_expect(__any(umax4(of[j & 7], 0u) == 0xffffffffu), 0)) {
#pragma unroll 1
          for (int i = 0; i < SPIN_MAX; ++i) {
            if (j < 8 && lword(M_ADONE) < 4) __builtin_amdgcn_s_sleep(16);
#pragma unroll
            for (int u = j; u < je; ++u) OLD(u)
            asm volatile("s_waitcnt vmcnt(%1)" : "+v"(of[j & 7]) : "i"(je - 1 - j) : "memory");
            if (!__any(umax4(of[j & 7], 0u) == 0xffffffffu)) break;
          }
        }
        c3 = MFMA(f[j % 3], of[j & 7], j ? c3 : zero16());
        SB;
        if (j + 3 < 16) f[j % 3] = ltr(wb1 + WOA(j + 3), 1024);
        SB;
        if (j < 8) C2STEP(j)
        if (j >= 8 + LAG) C2STEP(j - LAG)
      }
      SB;
#pragma unroll
      for (int j = 16 - LAG; j < 16; ++j) {
        C2STEP(j)
        if (j == 17 - LAG) {
#pragma unroll
          for (int i = 0; i < 16; ++i) c1[i] += g * c3[i];
          XWR(c1, 0) larrive(M_PSYNC + rb * 4);
        }
        SB;
      }
    }
    {
      const unsigned xo1 = L_RG + (m ^ 1) * RGN + 16384 + l16;
      f32x4 pv[4];
#pragma unroll 1
      for (int i = 0; i < (SPIN_MAX << 4); ++i) {
        const unsigned cv = __hip_atomic_load(lp<unsigned>(M_PSYNC + rb * 4), __ATOMIC_RELAXED, __HIP_MEMORY_SCOPE_WORKGROUP);
        asm volatile("" ::: "memory");
#pragma unroll
        for (int q = 0; q < 4; ++q) pv[q] = *lp<f32x4>(xo1 + q * 1024);
        asm volatile("" ::: "memory");
        if (__builtin_amdgcn_readfirstlane(cv) >= 2) break;
      }
#pragma unroll
      for (int i = 0; i < 16; ++i) c0[i] += g * c2[i] + pv[i >> 2][i & 3];
      unsigned o[8];
#pragma unroll
      for (int i = 0; i < 8; ++i) o[i] = pk2(c0[2 * i], c0[2 * i + 1]);
      const u32x4 sout = make_srd(out, 2048 * 512 * 2);
      const unsigned vo = r * 1024 + hh * 16;
      const unsigned so = (64 * rt + 32 * rb) * 1024 + (64 * ct + 32 * cb) * 2;
#pragma unroll
      for (int k = 0; k < 2; ++k) {
        auto rx = __builtin_amdgcn_permlane32_swap(o[4 * k], o[4 * k + 2], false, false);
        auto ry = __builtin_amdgcn_permlane32_swap(o[4 * k + 1], o[4 * k + 3], false, false);
        u32x4 v = {rx[0], ry[0], rx[1], ry[1]};
        st16(v, sout, vo + k * 32, so);
      }
    }
    if (blockIdx.x == 0 && m == 0 && lane == 0) st4c(tag, sws, 0, WS_E);
  }
}

extern "C" void kernel_launch(void* const* d_in, const int* in_sizes, int n_in, void* d_out, int out_size, void* d_ws,
                              size_t ws_size, hipStream_t stream) {
  gm_kernel<<<256, 768, 0, stream>>>((const u16*)d_in[0], (const u16*)d_in[1], d_ws, (const u16*)d_in[4], (const u16*)d_in[5],
                                     (const u16*)d_in[6], (const u16*)d_in[2], (const u16*)d_in[8], (const u16*)d_in[7], (u16*)d_out);
}
```
